```python
import torch

def _softmax(x, axis=-1):
    return torch.softmax(x, dim=axis)
def _gelu(x):
    return torch.nn.functional.gelu(x, approximate="tanh")
def _silu(x):
    return torch.nn.functional.silu(x)
def _sigmoid(x):
    return torch.sigmoid(x)
def _layernorm(x, axis=-1, eps=1e-5):
    m = x.mean(dim=axis, keepdim=True)
    v = x.var(dim=axis, keepdim=True, unbiased=False)
    return (x - m) / torch.sqrt(v + eps)
def _scatter_add(tgt, idx, src, axis=0):
    return tgt.clone().index_add_(axis, idx, src)
def _segment_sum(src, seg, n):
    out = torch.zeros((n,) + src.shape[1:], dtype=src.dtype)
    return out.index_add_(0, seg, src)
def _pad(x, pw):
    flat = tuple(v for pair in reversed(pw) for v in pair)
    return torch.nn.functional.pad(x, flat)

kernel_name = 'concat_heads__gqa_block_cached__gqa_block_cached__near_ridge'

def setup_inputs():

    _w = (lambda a: (lambda t: t.to(torch.bfloat16) if t.is_floating_point() else t)(torch.as_tensor(a)))
    def _A_setup():
        B, Sq, D = 16, 144, 640
        Hq, Hkv, Sk, Dh = 32, 32, 512, 128
        assert Hq % Hkv == 0, (Hq, Hkv)
        return {
            "x": _w((lambda *s: torch.randn(*s, dtype=torch.bfloat16) * 0.8)(B, Sq, D)),
            "k_cache": _w((lambda *s: torch.randn(*s, dtype=torch.bfloat16) * 0.8)(B, Hkv, Sk, Dh)),
            "v_cache": _w((lambda *s: torch.randn(*s, dtype=torch.bfloat16) * 0.05)(B, Hkv, Sk, Dh)),
            "wq": _w((lambda *s: torch.randn(*s, dtype=torch.bfloat16) * 0.8)(D, Hq * Dh) * (D ** -0.5)),
            "wo": _w((lambda *s: torch.randn(*s, dtype=torch.bfloat16) * 0.05)(Hq * Dh, D) * ((Hq * Dh) ** -0.5)),
        }
    _A_in = _A_setup()

    def _B_setup():
        B, Sq, D = 16, 144, 640
        Hq, Hkv, Sk, Dh = 32, 32, 512, 128
        assert Hq % Hkv == 0, (Hq, Hkv)
        return {
            "x": _w((lambda *s: torch.randn(*s, dtype=torch.bfloat16) * 0.8)(B, Sq, D)),
            "k_cache": _w((lambda *s: torch.randn(*s, dtype=torch.bfloat16) * 0.8)(B, Hkv, Sk, Dh)),
            "v_cache": _w((lambda *s: torch.randn(*s, dtype=torch.bfloat16) * 0.05)(B, Hkv, Sk, Dh)),
            "wq": _w((lambda *s: torch.randn(*s, dtype=torch.bfloat16) * 0.8)(D, Hq * Dh) * (D ** -0.5)),
            "wo": _w((lambda *s: torch.randn(*s, dtype=torch.bfloat16) * 0.05)(Hq * Dh, D) * ((Hq * Dh) ** -0.5)),
        }
    _B_in = _B_setup()

    return {
        "A_x": _A_in["x"],
        "A_k_cache": _A_in["k_cache"],
        "A_v_cache": _A_in["v_cache"],
        "A_wq": _A_in["wq"],
        "A_wo": _A_in["wo"],
        "B_x": _B_in["x"],
        "B_k_cache": _B_in["k_cache"],
        "B_v_cache": _B_in["v_cache"],
        "B_wq": _B_in["wq"],
        "B_wo": _B_in["wo"],
    }

def reference(A_x, A_k_cache, A_v_cache, A_wq, A_wo, B_x, B_k_cache, B_v_cache, B_wq, B_wo):
    def _A_ref(x, k_cache, v_cache, wq, wo):
        B, Sq, D = x.shape
        Hq, Dh = 32, 128
        Hkv = k_cache.shape[1]
        rep = Hq // Hkv
        q = (lambda x, *d: x.permute(*d))((x @ wq).reshape(B, Sq, Hq, Dh), 0, 2, 1, 3)
        k = (lambda x, n, axis: torch.repeat_interleave(x, n, dim=axis))(k_cache, rep, axis=1)
        v = (lambda x, n, axis: torch.repeat_interleave(x, n, dim=axis))(v_cache, rep, axis=1)
        scores = torch.einsum("bhqd,bhkd->bhqk", q, k) * (Dh ** -0.5)
        p = _softmax(scores, axis=-1)
        o = torch.einsum("bhqk,bhkd->bhqd", p, v)
        return (lambda x, *d: x.permute(*d))(o, 0, 2, 1, 3).reshape(B, Sq, Hq * Dh) @ wo

    def _B_ref(x, k_cache, v_cache, wq, wo):
        B, Sq, D = x.shape
        Hq, Dh = 32, 128
        Hkv = k_cache.shape[1]
        rep = Hq // Hkv
        q = (lambda x, *d: x.permute(*d))((x @ wq).reshape(B, Sq, Hq, Dh), 0, 2, 1, 3)
        k = (lambda x, n, axis: torch.repeat_interleave(x, n, dim=axis))(k_cache, rep, axis=1)
        v = (lambda x, n, axis: torch.repeat_interleave(x, n, dim=axis))(v_cache, rep, axis=1)
        scores = torch.einsum("bhqd,bhkd->bhqk", q, k) * (Dh ** -0.5)
        p = _softmax(scores, axis=-1)
        o = torch.einsum("bhqk,bhkd->bhqd", p, v)
        return (lambda x, *d: x.permute(*d))(o, 0, 2, 1, 3).reshape(B, Sq, Hq * Dh) @ wo

    a = _A_ref(A_x, A_k_cache, A_v_cache, A_wq, A_wo)
    b = _B_ref(B_x, B_k_cache, B_v_cache, B_wq, B_wo)
    return torch.concatenate([a, b], axis=-1)
```

```cpp
#include <hip/hip_runtime.h>
#include <hip/hip_bf16.h>
#include <stdint.h>

typedef __bf16 bf16x8 __attribute__((ext_vector_type(8)));
typedef __bf16 bf16x4 __attribute__((ext_vector_type(4)));
typedef __bf16 bf16x2 __attribute__((ext_vector_type(2)));
typedef float f32x4 __attribute__((ext_vector_type(4)));
typedef float f32x2 __attribute__((ext_vector_type(2)));
typedef unsigned u32x4 __attribute__((ext_vector_type(4)));
typedef unsigned u32x2 __attribute__((ext_vector_type(2)));
#define LDSP __attribute__((address_space(3)))
#define DI __device__ __forceinline__
typedef __amdgpu_buffer_rsrc_t rsrc_t;

constexpr int NBATCH = 16, SQ = 144, DM = 640, NH = 32, SK = 512, DH = 128, HD = NH * DH;
constexpr float QSCALE = 0.08838834764831845f * 1.4426950408889634f;
constexpr float LTHR = 65536.f;

constexpr unsigned BUFA = 0, BUFB = 40960;
constexpr unsigned SL0 = 0, SL1 = 16384, SL2 = 40960, SL3 = 57344;
constexpr unsigned SCR0 = 34816;
constexpr unsigned MRG_ML = 34816, MRG_IN = 36864, MRG_IN3 = 73728;
constexpr unsigned K1_LDS = 81920;

DI f32x4 mfma16(bf16x8 a, bf16x8 b, f32x4 c) { return __builtin_amdgcn_mfma_f32_16x16x32_bf16(a, b, c, 0, 0, 0); }
DI unsigned cvtpk(float lo, float hi) { f32x2 v = {lo, hi}; return __builtin_bit_cast(unsigned, __builtin_convertvector(v, bf16x2)); }
DI bf16x8 lds_b128(unsigned addr) { return *(LDSP const bf16x8*)(size_t)addr; }
DI bf16x4 lds_tr(unsigned addr) { return __builtin_amdgcn_ds_read_tr16_b64_v4bf16((LDSP bf16x4*)(size_t)addr); }
DI bf16x8 cat(bf16x4 a, bf16x4 b) { return __builtin_shufflevector(a, b, 0, 1, 2, 3, 4, 5, 6, 7); }
DI rsrc_t mkrsrc(const void* p, unsigned bytes) { return __builtin_amdgcn_make_buffer_rsrc((void*)p, (short)0, (int)bytes, 0x00020000); }

template <unsigned M0V, int OFF, bool NT> DI void dma(unsigned voff, rsrc_t srd, unsigned soff) {
  if constexpr (NT) asm volatile("s_mov_b32 m0, %0\n\ts_nop 0\n\tbuffer_load_dwordx4 %1, %2, %3 offen offset:%4 nt lds" :: "n"(M0V), "v"(voff), "s"(srd), "s"(soff), "n"(OFF) : "memory");
  else asm volatile("s_mov_b32 m0, %0\n\ts_nop 0\n\tbuffer_load_dwordx4 %1, %2, %3 offen offset:%4 lds" :: "n"(M0V), "v"(voff), "s"(srd), "s"(soff), "n"(OFF) : "memory");
}
template <int N> DI void wait_bar() { asm volatile("s_waitcnt vmcnt(%0) lgkmcnt(0)\n\ts_barrier" :: "n"(N) : "memory"); }

DI float xsum4(float v) {
  auto a = __builtin_amdgcn_permlane32_swap(__float_as_uint(v), __float_as_uint(v), false, false);
  v = __uint_as_float(a[0]) + __uint_as_float(a[1]);
  auto b = __builtin_amdgcn_permlane16_swap(__float_as_uint(v), __float_as_uint(v), false, false);
  return __uint_as_float(b[0]) + __uint_as_float(b[1]);
}
DI float xmax4(float v) {
  auto a = __builtin_amdgcn_permlane32_swap(__float_as_uint(v), __float_as_uint(v), false, false);
  v = fmaxf(__uint_as_float(a[0]), __uint_as_float(a[1]));
  auto b = __builtin_amdgcn_permlane16_swap(__float_as_uint(v), __float_as_uint(v), false, false);
  return fmaxf(__uint_as_float(b[0]), __uint_as_float(b[1]));
}
DI bf16x8 pack_nat(f32x4 x, f32x4 y, float sc) {
  unsigned x0 = cvtpk(x[0] * sc, x[1] * sc), x1 = cvtpk(x[2] * sc, x[3] * sc);
  unsigned y0 = cvtpk(y[0] * sc, y[1] * sc), y1 = cvtpk(y[2] * sc, y[3] * sc);
  auto p0 = __builtin_amdgcn_permlane32_swap(x0, y0, false, false);
  auto p1 = __builtin_amdgcn_permlane32_swap(x1, y1, false, false);
  auto q0 = __builtin_amdgcn_permlane16_swap(p0[0], p0[1], false, false);
  auto q1 = __builtin_amdgcn_permlane16_swap(p1[0], p1[1], false, false);
  u32x4 r = {q0[0], q1[0], q0[1], q1[1]};
  return __builtin_bit_cast(bf16x8, r);
}
DI bf16x8 pack_acc(f32x4 x, f32x4 y, float sc) {
  u32x4 r = {cvtpk(x[0] * sc, x[1] * sc), cvtpk(x[2] * sc, x[3] * sc), cvtpk(y[0] * sc, y[1] * sc), cvtpk(y[2] * sc, y[3] * sc)};
  return __builtin_bit_cast(bf16x8, r);
}


#define SB() __builtin_amdgcn_sched_barrier(0)
#define OPAQUE(x) asm volatile("" : "+v"(x))
DI unsigned lane_id() { unsigned l; asm volatile("v_mbcnt_lo_u32_b32 %0, -1, 0\n\tv_mbcnt_hi_u32_b32 %0, -1, %0" : "=v"(l)); return l; }
DI float fadd(float a, float b) { float r; asm("v_add_f32_e32 %0, %1, %2" : "=v"(r) : "v"(a), "v"(b)); return r; }

struct K1State {
  unsigned kb, vb0, vb1, voffKV;
  bf16x8 Qf[3][4];
  f32x4 O[3][8];
  f32x4 negm[3];
  float l[3];
};
template <int W> DI unsigned mk_voffW(unsigned lane) { const unsigned r8 = (lane >> 2) & 7; return r8 * 8192 + (lane >> 5) * 64 + (((lane & 3) ^ (2 * (((r8 >> 2) ^ W) & 1))) * 16); }
DI unsigned mk_voffX(unsigned lane) { const unsigned r8 = (lane >> 2) & 7; return r8 * 1280 + (lane >> 5) * 64 + (((lane & 3) ^ (2 * ((r8 >> 2) & 1))) * 16); }

template <int W, unsigned BUF> DI void issue_qw(unsigned voffW, rsrc_t srdW, unsigned soffW) {
  dma<BUF + 2048 * W, 0, false>(voffW, srdW, soffW + (8 * W) * 8192);
  dma<BUF + 2048 * W + 1024, 0, false>(voffW, srdW, soffW + (8 * W) * 8192 + 128);
  dma<BUF + 2048 * (W + 4), 0, false>(voffW, srdW, soffW + (8 * (W + 4)) * 8192);
  dma<BUF + 2048 * (W + 4) + 1024, 0, false>(voffW, srdW, soffW + (8 * (W + 4)) * 8192 + 128);
}
template <int W, unsigned BUF> DI void issue_qx(unsigned voffX, rsrc_t srdX, unsigned soffX) {
  dma<BUF + 16384 + 1024 * W, 0, false>(voffX, srdX, soffX + (8 * W) * 1280);
  dma<BUF + 16384 + 1024 * (W + 4), 0, false>(voffX, srdX, soffX + (8 * (W + 4)) * 1280);
  dma<BUF + 16384 + 1024 * (W + 8), 0, false>(voffX, srdX, soffX + (8 * (W + 8)) * 1280);
  dma<BUF + 16384 + 1024 * (W + 12), 0, false>(voffX, srdX, soffX + (8 * (W + 12)) * 1280);
  if constexpr (W < 2) dma<BUF + 16384 + 1024 * (W + 16), 0, false>(voffX, srdX, soffX + (8 * (W + 16)) * 1280);
}
template <int W, unsigned SLOT> DI void issue_kv(const K1State& c, rsrc_t srd, unsigned tileoff) {
  dma<SLOT + 4096 * W, 0, true>(c.voffKV, srd, tileoff + 4096 * W);
  dma<SLOT + 4096 * W, 2048, true>(c.voffKV, srd, tileoff + 4096 * W);
  dma<SLOT + 4096 * W + 1024, 0, true>(c.voffKV, srd, tileoff + 4096 * W + 128);
  dma<SLOT + 4096 * W + 1024, 2048, true>(c.voffKV, srd, tileoff + 4096 * W + 128);
}

template <unsigned BUF> DI bf16x8 w_frag(unsigned wb0, unsigned wb1, int idx) {
  const int s = idx >> 3, dt = idx & 7;
  const unsigned o = BUF + 8192 * s + 512 * (dt >> 1);
  return cat(lds_tr(((dt & 1) ? wb1 : wb0) + o), lds_tr(((dt & 1) ? wb0 : wb1) + o + 256));
}
template <int W, unsigned BUF> DI void q_stage(unsigned xb, unsigned wb0, unsigned wb1, f32x4 (&acc)[8][2], f32x4 (&accL)[2]) {
  constexpr int PF = 5;
  bf16x8 xf[2][3], wf[16];
  xf[0][0] = lds_b128(xb + BUF + 16384 + 2048 * (2 * W));
  wf[0] = w_frag<BUF>(wb0, wb1, 0);
  xf[0][1] = lds_b128(xb + BUF + 16384 + 2048 * (2 * W + 1));
  xf[0][2] = lds_b128(xb + BUF + 16384 + 2048 * 8);
#pragma unroll
  for (int i = 1; i < PF; ++i) wf[i] = w_frag<BUF>(wb0, wb1, i);
  SB();
#pragma unroll
  for (int idx = 0; idx < 16; ++idx) {
    const int s = idx >> 3, dt = idx & 7;
    acc[dt][0] = mfma16(wf[idx], xf[s][0], acc[dt][0]);
    if (idx + PF < 16) wf[idx + PF] = w_frag<BUF>(wb0, wb1, idx + PF);
    if (idx == 1) xf[1][0] = lds_b128(xb + BUF + 16384 + 2048 * (2 * W) + 512);
    if (idx == 2) xf[1][1] = lds_b128(xb + BUF + 16384 + 2048 * (2 * W + 1) + 512);
    if (idx == 3) xf[1][2] = lds_b128(xb + BUF + 16384 + 2048 * 8 + 512);
    acc[dt][1] = mfma16(wf[idx], xf[s][1], acc[dt][1]);
    if ((dt >> 1) == W) accL[dt & 1] = mfma16(wf[idx], xf[s][2], accL[dt & 1]);
    SB();
  }
}

struct SMState { float e[3][8]; float a[3][4]; };
template <int NQ> DI void sm_step(K1State& c, SMState& m, const f32x4 (&S)[2][3], bf16x8 (&P)[3], int step) {
  const int t = step / NQ, qi = step % NQ;
  if (step >= 2 * NQ) return;
#pragma unroll
  for (int i = 0; i < 4; ++i) m.e[qi][4 * t + i] = __builtin_amdgcn_exp2f(S[t][qi][i]);
  u32x4 r = __builtin_bit_cast(u32x4, P[qi]);
  r[2 * t] = cvtpk(m.e[qi][4 * t], m.e[qi][4 * t + 1]);
  r[2 * t + 1] = cvtpk(m.e[qi][4 * t + 2], m.e[qi][4 * t + 3]);
  P[qi] = __builtin_bit_cast(bf16x8, r);
  c.l[qi] = fadd(c.l[qi], fadd(fadd(m.e[qi][4 * t], m.e[qi][4 * t + 1]), fadd(m.e[qi][4 * t + 2], m.e[qi][4 * t + 3])));
}

template <int W, unsigned SLOT, int NQ0, int NQ1, bool FIRST>
DI void k_phase(K1State& c, f32x4 (&S1)[2][3], bf16x8 (&P0)[3], bool redo) {
  constexpr int PF = 4;
  constexpr int NC0 = FIRST ? 3 : NQ0;
  f32x4 S0[2][3];
  bf16x8 kf[16];
  SMState m;
#pragma unroll
  for (int i = 0; i < PF; ++i) kf[i] = lds_b128(c.kb + SLOT + 4096 * (i >> 2) + 512 * (i & 3));
  SB();
#pragma unroll
  for (int idx = 0; idx < 16; ++idx) {
    const int j = idx >> 3, t = (idx >> 2) & 1, s = idx & 3;
    if (j == 0) {
#pragma unroll
      for (int qi = 0; qi < NC0; ++qi) S0[t][qi] = mfma16(kf[idx], c.Qf[qi][s], s == 0 ? c.negm[qi] : S0[t][qi]);
    } else {
#pragma unroll
      for (int qi = 0; qi < NQ1; ++qi) S1[t][qi] = mfma16(kf[idx], c.Qf[qi][s], s == 0 ? c.negm[qi] : S1[t][qi]);
    }
    if (idx + PF < 16) kf[idx + PF] = lds_b128(c.kb + SLOT + 4096 * ((idx + PF) >> 2) + 512 * ((idx + PF) & 3));
    if (!FIRST) {
      if (idx >= 6 && idx < 6 + NQ0) sm_step<NQ0>(c, m, S0, P0, idx - 6);
      if (idx >= 10 && idx < 10 + NQ0) sm_step<NQ0>(c, m, S0, P0, NQ0 + idx - 10);
    }
    SB();
  }
  if (FIRST) {
#pragma unroll
    for (int qi = 0; qi < 3; ++qi) {
      float mx = fmaxf(fmaxf(fmaxf(S0[0][qi][0], S0[0][qi][1]), fmaxf(S0[0][qi][2], S0[0][qi][3])), fmaxf(fmaxf(S0[1][qi][0], S0[1][qi][1]), fmaxf(S0[1][qi][2], S0[1][qi][3])));
      if (qi < 2) mx = fmaxf(mx, fmaxf(fmaxf(fmaxf(S1[0][qi][0], S1[0][qi][1]), fmaxf(S1[0][qi][2], S1[0][qi][3])), fmaxf(fmaxf(S1[1][qi][0], S1[1][qi][1]), fmaxf(S1[1][qi][2], S1[1][qi][3]))));
      mx = xmax4(mx);
      const float dl = redo ? 0.f : mx;
      c.negm[qi] -= dl;
      if (qi < NQ0) { S0[0][qi] -= dl; S0[1][qi] -= dl; }
      if (qi < NQ1) { S1[0][qi] -= dl; S1[1][qi] -= dl; }
    }
#pragma unroll
    for (int st = 0; st < 2 * NQ0; ++st) sm_step<NQ0>(c, m, S0, P0, st);
    SB();
  }
}
template <unsigned SLOT> DI bf16x8 v_frag(const K1State& c, int idx) {
  const int j = idx >> 3, dt = idx & 7;
  const unsigned o = SLOT + 8192 * j + 512 * (dt >> 1);
  const unsigned vb = (dt & 1) ? c.vb1 : c.vb0;
  return cat(lds_tr(vb + o), lds_tr(vb + o + 4096));
}
template <int W, unsigned SLOT, int NQ0, int NQ1>
DI void v_phase(K1State& c, const f32x4 (&S1)[2][3], const bf16x8 (&P0)[3]) {
  constexpr int PF = 4;
  bf16x8 vf[16], P1[3];
  SMState m;
#pragma unroll
  for (int i = 0; i < PF; ++i) vf[i] = v_frag<SLOT>(c, i);
  sm_step<NQ1>(c, m, S1, P1, 0);
  SB();
#pragma unroll
  for (int idx = 0; idx < 16; ++idx) {
    const int j = idx >> 3, dt = idx & 7;
    if (j == 0) {
#pragma unroll
      for (int qi = 0; qi < NQ0; ++qi) c.O[qi][dt] = mfma16(vf[idx], P0[qi], c.O[qi][dt]);
    } else {
#pragma unroll
      for (int qi = 0; qi < NQ1; ++qi) c.O[qi][dt] = mfma16(vf[idx], P1[qi], c.O[qi][dt]);
    }
    if (idx + PF < 16) vf[idx + PF] = v_frag<SLOT>(c, idx + PF);
    if (idx < 2 * NQ1 - 1) sm_step<NQ1>(c, m, S1, P1, idx + 1);
    SB();
  }
}

struct Ptrs { const char* x[2]; const char* k[2]; const char* v[2]; const char* wq[2]; const char* wo[2]; char* o; char* out; };

template <int W> DI void k1_wave(const Ptrs& p) {
  K1State c;
  const unsigned bid = blockIdx.x, xcd = bid & 7, loc = bid >> 3;
  const unsigned h = (xcd & 3) * 8 + (loc & 7), b = (xcd >> 2) * 8 + (loc >> 3);
  constexpr int NQX = W < 2 ? 5 : 4;
  constexpr int NA0 = W == 0 ? 3 : 2, NA1 = W == 1 ? 3 : 2, NB0 = W == 2 ? 3 : 2, NB1 = W == 3 ? 3 : 2;
  const unsigned soffW0 = h * 256;

  rsrc_t srdW = mkrsrc(p.wq[0], DM * HD * 2), srdX = mkrsrc(p.x[0] + (size_t)b * SQ * DM * 2, SQ * DM * 2);
  {
    const unsigned lane = lane_id();
    issue_qw<W, BUFA>(mk_voffW<W>(lane), srdW, soffW0);
    issue_qx<W, BUFA>(mk_voffX(lane), srdX, 0);
  }

#pragma unroll 1
  for (int br = 0; br < 2; ++br) {
    const size_t kvoff = ((size_t)b * NH + h) * SK * DH * 2;
    const rsrc_t srdK = mkrsrc((br ? p.k[1] : p.k[0]) + kvoff, SK * DH * 2), srdV = mkrsrc((br ? p.v[1] : p.v[0]) + kvoff, SK * DH * 2);
    {
      const unsigned lane = lane_id();
      const unsigned r = lane & 15, g = lane >> 4, qq = (lane & 15) >> 2, pp = lane & 3;
      unsigned xb = 1024 * (r >> 3) + 64 * (r & 7) + 16 * (g ^ (2 * ((r >> 2) & 1)));
      unsigned wb0 = 2048 * g + 64 * qq + 16 * ((pp >> 1) ^ (2 * (g & 1))) + 8 * (pp & 1);
      unsigned wb1 = wb0 ^ 32;
      unsigned voffW = mk_voffW<W>(lane), voffX = mk_voffX(lane);
      c.kb = 2048 * (r >> 3) + 64 * (r & 7) + 16 * (g ^ (2 * ((r >> 2) & 1)));
      c.vb0 = 2048 * (g >> 1) + 64 * (4 * (g & 1) + qq) + 16 * ((pp >> 1) ^ (2 * (g & 1))) + 8 * (pp & 1) + 16384;
      c.vb1 = c.vb0 ^ 32;
      { const unsigned r8 = (lane >> 2) & 7; c.voffKV = r8 * 256 + (lane >> 5) * 64 + (((lane & 3) ^ (2 * ((r8 >> 2) & 1))) * 16); }
      xb += 16384;
      OPAQUE(xb); OPAQUE(wb0); OPAQUE(wb1); OPAQUE(c.kb); OPAQUE(c.vb0); OPAQUE(c.vb1);
      xb -= 16384; c.vb0 -= 16384; c.vb1 -= 16384;
      f32x4 acc[8][2], accL[2];
#pragma unroll
      for (int i = 0; i < 8; ++i) { acc[i][0] = f32x4{}; acc[i][1] = f32x4{}; }
      accL[0] = f32x4{}; accL[1] = f32x4{};
#pragma unroll 1
      for (int j = 0; j < 10; j += 2) {
        wait_bar<0>();
        issue_qw<W, BUFB>(voffW, srdW, soffW0 + (j + 1) * 64 * 8192); issue_qx<W, BUFB>(voffX, srdX, (j + 1) * 128);
        q_stage<W, BUFA>(xb, wb0, wb1, acc, accL);
        wait_bar<0>();
        if (j < 8) { issue_qw<W, BUFA>(voffW, srdW, soffW0 + (j + 2) * 64 * 8192); issue_qx<W, BUFA>(voffX, srdX, (j + 2) * 128); }
        else { issue_kv<W, SL0>(c, srdK, 0); issue_kv<W, SL1>(c, srdV, 0); }
        q_stage<W, BUFB>(xb, wb0, wb1, acc, accL);
      }
#pragma unroll
      for (int s = 0; s < 4; ++s) { c.Qf[0][s] = pack_nat(acc[2 * s][0], acc[2 * s + 1][0], QSCALE); c.Qf[1][s] = pack_nat(acc[2 * s][1], acc[2 * s + 1][1], QSCALE); }
      *(LDSP bf16x8*)(size_t)(SCR0 + 1024 * W + 16 * lane_id()) = pack_nat(accL[0], accL[1], QSCALE);
    }
    srdW = mkrsrc(p.wq[1], br == 0 ? DM * HD * 2 : 0); srdX = mkrsrc(p.x[1] + (size_t)b * SQ * DM * 2, br == 0 ? SQ * DM * 2 : 0);
#pragma unroll
    for (int qi = 0; qi < 3; ++qi) c.negm[qi] = f32x4{};
    bool redo = false;
    wait_bar<4>();
    {
      unsigned qa = SCR0 + 16 * lane_id(); OPAQUE(qa);
#pragma unroll
      for (int s = 0; s < 4; ++s) c.Qf[2][s] = lds_b128(qa + 1024 * s);
    }
#pragma unroll 1
    for (;;) {
#pragma unroll
      for (int qi = 0; qi < 3; ++qi) {
        c.l[qi] = 0.f;
#pragma unroll
        for (int dt = 0; dt < 8; ++dt) c.O[qi][dt] = f32x4{};
      }
      f32x4 S1[2][3]; bf16x8 P0[3];
      issue_kv<W, SL2>(c, srdK, 16384); issue_kv<W, SL3>(c, srdV, 16384);
      k_phase<W, SL0, NA0, NA1, true>(c, S1, P0, redo);
#pragma unroll 1
      for (int it = 0;; ++it) {
        const unsigned to = (unsigned)it * 32768;
        wait_bar<8>();
        if (it < 3) issue_kv<W, SL0>(c, srdK, to + 32768); else issue_qw<W, BUFA>(mk_voffW<W>(lane_id()), srdW, soffW0);
        v_phase<W, SL1, NA0, NA1>(c, S1, P0);
        wait_bar<8>();
        if (it < 3) issue_kv<W, SL1>(c, srdV, to + 32768); else issue_qx<W, BUFA>(mk_voffX(lane_id()), srdX, 0);
        k_phase<W, SL2, NB0, NB1, false>(c, S1, P0, false);
        if (it < 3) { wait_bar<8>(); issue_kv<W, SL2>(c, srdK, to + 49152); } else wait_bar<4 + NQX>();
        v_phase<W, SL3, NB0, NB1>(c, S1, P0);
        if (it == 3) break;
        wait_bar<8>();
        issue_kv<W, SL3>(c, srdV, to + 49152);
        k_phase<W, SL0, NA0, NA1, false>(c, S1, P0, false);
      }
      const unsigned lane = lane_id();
      char* ob = p.o + ((size_t)(br * NBATCH + b) * 9 * 128 + 4 * h) * 1024 + lane * 16;
      const float l0 = xsum4(c.l[0]), l1 = xsum4(c.l[1]), lL = xsum4(c.l[2]);
      const bool bad = __any(!(fmaxf(fmaxf(l0, l1), lL) <= LTHR) || !(l0 == l0) || !(l1 == l1) || !(lL == lL));
      unsigned wa = 16 * lane; OPAQUE(wa);
#pragma unroll
      for (int o = 0; o < 4; ++o) {
        if (o == W) continue;
        const unsigned src = W < o ? W : W - 1;
        const unsigned dst = (o < 3 ? MRG_IN + 6144 * o : MRG_IN3 - 16384) + 2048 * src;
        *(LDSP f32x4*)(size_t)(wa + (o < 3 ? 0 : 16384) + dst) = c.O[2][2 * o];
        *(LDSP f32x4*)(size_t)(wa + (o < 3 ? 0 : 16384) + dst + 1024) = c.O[2][2 * o + 1];
      }
      if (lane < 16) { f32x2 ml = {lL, bad ? 1.f : 0.f}; *(LDSP f32x2*)(size_t)(MRG_ML + 128 * W + 8 * lane) = ml; }
      {
        const float inv0 = __builtin_amdgcn_rcpf(l0), inv1 = __builtin_amdgcn_rcpf(l1);
#pragma unroll
        for (int s = 0; s < 4; ++s) *(bf16x8*)(ob + (size_t)((2 * W) * 128 + s) * 1024) = pack_acc(c.O[0][2 * s], c.O[0][2 * s + 1], inv0);
#pragma unroll
        for (int s = 0; s < 4; ++s) *(bf16x8*)(ob + (size_t)((2 * W + 1) * 128 + s) * 1024) = pack_acc(c.O[1][2 * s], c.O[1][2 * s + 1], inv1);
      }
      asm volatile("s_waitcnt lgkmcnt(0)\n\ts_barrier" ::: "memory");
      f32x2 ml[4];
#pragma unroll
      for (int o = 0; o < 4; ++o) ml[o] = *(LDSP const f32x2*)(size_t)(MRG_ML + 128 * o + 8 * (lane & 15));
      f32x4 a0 = c.O[2][2 * W], a1 = c.O[2][2 * W + 1];
#pragma unroll
      for (int o = 0; o < 3; ++o) {
        const unsigned ad = (W < 3 ? MRG_IN + 6144 * W : MRG_IN3) + 2048 * o;
        a0 += *(LDSP const f32x4*)(size_t)(wa + ad);
        a1 += *(LDSP const f32x4*)(size_t)(wa + ad + 1024);
      }
      const float L = (ml[0][0] + ml[1][0]) + (ml[2][0] + ml[3][0]);
      *(bf16x8*)(ob + (size_t)(8 * 128 + W) * 1024) = pack_acc(a0, a1, __builtin_amdgcn_rcpf(L));
      const bool anybad = __builtin_amdgcn_readfirstlane(__float_as_uint((ml[0][1] + ml[1][1]) + (ml[2][1] + ml[3][1]))) != 0u;
      if (__builtin_expect(!anybad || redo, 1)) break;
      redo = true;
      float mx[3] = {-INFINITY, -INFINITY, -INFINITY};
#pragma unroll 1
      for (int t = 0; t < 8; ++t) {
        wait_bar<0>();
        issue_kv<W, SL0>(c, srdK, t * 16384);
        wait_bar<0>();
#pragma unroll 1
        for (int a = 0; a < 4; ++a) {
          f32x4 s[3] = {f32x4{}, f32x4{}, f32x4{}};
#pragma unroll
          for (int ks = 0; ks < 4; ++ks) {
            const bf16x8 kf = lds_b128(c.kb + SL0 + 4096 * a + 512 * ks);
#pragma unroll
            for (int qi = 0; qi < 3; ++qi) s[qi] = mfma16(kf, c.Qf[qi][ks], s[qi]);
          }
#pragma unroll
          for (int qi = 0; qi < 3; ++qi) mx[qi] = fmaxf(fmaxf(mx[qi], fmaxf(s[qi][0], s[qi][1])), fmaxf(s[qi][2], s[qi][3]));
        }
      }
#pragma unroll
      for (int qi = 0; qi < 3; ++qi) { const float m = -xmax4(mx[qi]); c.negm[qi] = f32x4{m, m, m, m}; }
      wait_bar<0>();
      issue_kv<W, SL0>(c, srdK, 0); issue_kv<W, SL1>(c, srdV, 0);
      wait_bar<4>();
    }
  }
}

__global__ __launch_bounds__(256, 2) void k1_attn(Ptrs p) {
  const unsigned w = __builtin_amdgcn_readfirstlane(threadIdx.x >> 6);
  if (w == 0) k1_wave<0>(p);
  else if (w == 1) k1_wave<1>(p);
  else if (w == 2) k1_wave<2>(p);
  else k1_wave<3>(p);
}

constexpr unsigned K2_LDS = 147456;
template <int N> DI void wait_vm() { asm volatile("s_waitcnt vmcnt(%0)" :: "n"(N) : "memory"); }

__global__ __launch_bounds__(512, 2) void k2_oproj(Ptrs p) {
  const unsigned lane = threadIdx.x & 63, w = __builtin_amdgcn_readfirstlane(threadIdx.x >> 6);
  const unsigned kg = w & 3, qh = w >> 2;
  const unsigned r = lane & 15, g = lane >> 4, qq = (lane & 15) >> 2, pp = lane & 3;
  const unsigned bid = blockIdx.x, xcd = bid & 7, loc = bid >> 3;
  const unsigned rbg = xcd * 6 + loc / 5, cb = loc % 5;
  const unsigned br = rbg / 24, rb = rbg % 24;
  const unsigned vb0 = 2048 * (g >> 1) + 64 * (4 * (g & 1) + qq) + 16 * ((pp >> 1) ^ (2 * (g & 1))) + 8 * (pp & 1) + kg * 8192;
  const unsigned vb1 = vb0 ^ 32;
  const unsigned r8 = (lane >> 2) & 7, sub = lane >> 5, sl = lane & 3;
  const unsigned voffW = r8 * 1280 + sub * 64 + ((sl ^ (2 * ((r8 >> 2) & 1))) * 16);
  const rsrc_t srdW = mkrsrc((br ? p.wo[1] : p.wo[0]), HD * DM * 2);
  const rsrc_t srdO = mkrsrc(p.o + (size_t)br * NBATCH * 9 * 128 * 1024, NBATCH * 9 * 128 * 1024);
  auto issue = [&](int st) {
    const unsigned slot = (st & 3) * 32768 + w * 4096;
    const unsigned so = (st * 128 + 16 * w) * 1280 + cb * 256;
#pragma unroll
    for (int i = 0; i < 4; ++i) {
      const unsigned m0v = slot + (i >> 1) * 2048 + (i & 1) * 1024, sof = so + (i >> 1) * 8 * 1280 + (i & 1) * 128;
      asm volatile("s_mov_b32 m0, %0\n\ts_nop 0\n\tbuffer_load_dwordx4 %1, %2, %3 offen lds" :: "s"(m0v), "v"(voffW), "s"(srdW), "s"(sof) : "memory");
    }
  };
  const unsigned ovoff = lane * 16;
  const unsigned obase = ((rb * 6 + qh * 3) * 128 + kg) * 1024;
  u32x4 ob[4][3];
  auto oload = [&](int st, u32x4 (&d)[3]) {
#pragma unroll
    for (int qi = 0; qi < 3; ++qi) {
      const unsigned sof = obase + qi * 128 * 1024 + st * 4096;
      asm volatile("buffer_load_dwordx4 %0, %1, %2, %3 offen" : "=v"(d[qi]) : "v"(ovoff), "s"(srdO), "s"(sof) : "memory");
    }
  };
  f32x4 acc[8][3];
#pragma unroll
  for (int i = 0; i < 8; ++i) { acc[i][0] = f32x4{}; acc[i][1] = f32x4{}; acc[i][2] = f32x4{}; }
  issue(0); oload(0, ob[0]); issue(1); oload(1, ob[1]); issue(2); oload(2, ob[2]);
  auto step = [&](int st, u32x4 (&cur)[3], u32x4 (&nxt)[3], unsigned slotoff) {
    if (st < 30) asm volatile("s_waitcnt vmcnt(14) lgkmcnt(0)\n\ts_barrier" : "+v"(cur[0]), "+v"(cur[1]), "+v"(cur[2]) :: "memory");
    else if (st == 30) asm volatile("s_waitcnt vmcnt(7) lgkmcnt(0)\n\ts_barrier" : "+v"(cur[0]), "+v"(cur[1]), "+v"(cur[2]) :: "memory");
    else asm volatile("s_waitcnt vmcnt(0) lgkmcnt(0)\n\ts_barrier" : "+v"(cur[0]), "+v"(cur[1]), "+v"(cur[2]) :: "memory");
    if (st + 3 < 32) { issue(st + 3); oload(st + 3, nxt); }
#pragma unroll
    for (int nt = 0; nt < 8; ++nt) {
      const unsigned o = slotoff + 512 * (nt >> 1);
      const unsigned vb = (nt & 1) ? vb1 : vb0;
      const bf16x8 a = cat(lds_tr(vb + o), lds_tr(vb + o + 4096));
#pragma unroll
      for (int qi = 0; qi < 3; ++qi) acc[nt][qi] = mfma16(a, __builtin_bit_cast(bf16x8, cur[qi]), acc[nt][qi]);
    }
  };
#pragma unroll 1
  for (int st = 0; st < 32; st += 4) {
    step(st, ob[0], ob[3], 0);
    step(st + 1, ob[1], ob[0], 32768);
    step(st + 2, ob[2], ob[1], 65536);
    step(st + 3, ob[3], ob[2], 98304);
  }
  asm volatile("s_waitcnt lgkmcnt(0)\n\ts_barrier" ::: "memory");
#pragma unroll
  for (int o = 0; o < 4; ++o) {
    if (o == (int)kg) continue;
    const unsigned src = kg < (unsigned)o ? kg : kg - 1;
    const unsigned dst = ((qh * 4 + o) * 3 + src) * 6144 + lane * 16;
#pragma unroll
    for (int e = 0; e < 2; ++e)
#pragma unroll
      for (int qi = 0; qi < 3; ++qi) *(LDSP f32x4*)(size_t)(dst + (e * 3 + qi) * 1024) = acc[2 * o + e][qi];
  }
  asm volatile("s_waitcnt lgkmcnt(0)\n\ts_barrier" ::: "memory");
  f32x4 a[2][3];
#pragma unroll
  for (int e = 0; e < 2; ++e)
#pragma unroll
    for (int qi = 0; qi < 3; ++qi)
      a[e][qi] = kg == 0 ? acc[e][qi] : kg == 1 ? acc[2 + e][qi] : kg == 2 ? acc[4 + e][qi] : acc[6 + e][qi];
#pragma unroll
  for (int s = 0; s < 3; ++s)
#pragma unroll
    for (int e = 0; e < 2; ++e)
#pragma unroll
      for (int qi = 0; qi < 3; ++qi) a[e][qi] += *(LDSP const f32x4*)(size_t)(((qh * 4 + kg) * 3 + s) * 6144 + lane * 16 + (e * 3 + qi) * 1024);
#pragma unroll
  for (int qi = 0; qi < 3; ++qi) {
    const unsigned row = (rb * 6 + qh * 3 + qi) * 16 + r;
    *(bf16x8*)(p.out + (size_t)row * (2 * DM * 2) + (br * DM + cb * 128 + kg * 32 + g * 8) * 2) = pack_nat(a[0][qi], a[1][qi], 1.f);
  }
}

extern "C" void kernel_launch(void* const* d_in, const int* in_sizes, int n_in, void* d_out, int out_size, void* d_ws, size_t ws_size, hipStream_t stream) {
  Ptrs p;
  for (int i = 0; i < 2; ++i) {
    p.x[i] = (const char*)d_in[5 * i + 0]; p.k[i] = (const char*)d_in[5 * i + 1]; p.v[i] = (const char*)d_in[5 * i + 2];
    p.wq[i] = (const char*)d_in[5 * i + 3]; p.wo[i] = (const char*)d_in[5 * i + 4];
  }
  p.o = (char*)d_ws; p.out = (char*)d_out;
  (void)hipFuncSetAttribute((const void*)k1_attn, hipFuncAttributeMaxDynamicSharedMemorySize, K1_LDS);
  (void)hipFuncSetAttribute((const void*)k2_oproj, hipFuncAttributeMaxDynamicSharedMemorySize, K2_LDS);
  hipLaunchKernelGGL(k1_attn, dim3(512), dim3(256), K1_LDS, stream, p);
  hipLaunchKernelGGL(k2_oproj, dim3(240), dim3(512), K2_LDS, stream, p);
}
```

```cpp
#include <hip/hip_runtime.h>
#include <hip/hip_bf16.h>
#include <stdint.h>
#include <utility>

typedef __bf16 bf16x8 __attribute__((ext_vector_type(8)));
typedef __bf16 bf16x4 __attribute__((ext_vector_type(4)));
typedef __bf16 bf16x2 __attribute__((ext_vector_type(2)));
typedef float f32x4 __attribute__((ext_vector_type(4)));
typedef float f32x2 __attribute__((ext_vector_type(2)));
typedef unsigned u32x4 __attribute__((ext_vector_type(4)));
typedef unsigned u32x2 __attribute__((ext_vector_type(2)));
#define WARM 1
constexpr int NT0 = 2;
#define WOEND 1
#define LDSP __attribute__((address_space(3)))
#define DI __device__ __forceinline__
typedef __amdgpu_buffer_rsrc_t rsrc_t;

constexpr int NBATCH = 16, SQ = 144, DM = 640, NH = 32, SK = 512, DH = 128, HD = NH * DH;
constexpr float QSCALE = 0.08838834764831845f * 1.4426950408889634f;
constexpr float LTHR = 65536.f;

constexpr unsigned HALF = 81920;
constexpr unsigned SL0 = 0, SL1 = 16384, SL2 = 32768, SL3 = 49152;
constexpr unsigned XA0 = SL1, XA1 = HALF + SL1, XB0 = SL3, XB1 = HALF + SL3;
constexpr unsigned xtail(unsigned area) { return (area >= HALF ? HALF : 0) + (area % HALF == SL1 ? 65536 : 67584); }
constexpr unsigned JUNK = 77824, MRG_ML = 78848, MRG_FL = 79360;
constexpr unsigned mrg_in(int o, int src) { return 3 * o + src < 8 ? SL2 + 2048 * (3 * o + src) : 69632 + 2048 * (3 * o + src - 8); }
constexpr unsigned K1_LDS = 2 * HALF;

DI f32x4 mfma16(bf16x8 a, bf16x8 b, f32x4 c) { return __builtin_amdgcn_mfma_f32_16x16x32_bf16(a, b, c, 0, 0, 0); }
DI void MF(f32x4& acc, const bf16x8& a, const bf16x8& b) { asm volatile("v_mfma_f32_16x16x32_bf16 %0, %1, %2, %0" : "+v"(acc) : "v"(a), "v"(b)); }
DI void MF0(f32x4& d, const bf16x8& a, const bf16x8& b, const f32x4& c) { asm volatile("v_mfma_f32_16x16x32_bf16 %0, %1, %2, %3" : "=&v"(d) : "v"(a), "v"(b), "v"(c)); }
DI void MFZ(f32x4& d, const bf16x8& a, const bf16x8& b) { asm volatile("v_mfma_f32_16x16x32_bf16 %0, %1, %2, 0" : "=&v"(d) : "v"(a), "v"(b)); }
template <bool Z> DI void MFX(f32x4& d, const bf16x8& a, const bf16x8& b) { if constexpr (Z) MFZ(d, a, b); else MF(d, a, b); }
#define MFMA_FENCE() asm volatile("s_nop 7\n\ts_nop 1" ::: "memory")
DI unsigned cvtpk(float lo, float hi) { f32x2 v = {lo, hi}; return __builtin_bit_cast(unsigned, __builtin_convertvector(v, bf16x2)); }
DI float mul1(float a, float b) { float r = a * b; asm("" : "+v"(r)); return r; }
DI bf16x8 lds_b128(unsigned addr) { return *(LDSP const bf16x8*)(size_t)addr; }
DI bf16x4 lds_tr(unsigned addr) { return __builtin_amdgcn_ds_read_tr16_b64_v4bf16((LDSP bf16x4*)(size_t)addr); }
DI bf16x8 cat(bf16x4 a, bf16x4 b) { return __builtin_shufflevector(a, b, 0, 1, 2, 3, 4, 5, 6, 7); }
#define BYTES_(b) (b)
DI rsrc_t mkrsrc(const void* p, unsigned bytes) { bytes = BYTES_(bytes); return __builtin_amdgcn_make_buffer_rsrc((void*)p, (short)0, (int)bytes, 0x00020000); }
#define OST_AUX 16
#define OST1_AUX 16
template <int AUX = OST_AUX> DI void ost(rsrc_t srd, unsigned voff, unsigned off, bf16x8 v) { __builtin_amdgcn_raw_buffer_store_b128(__builtin_bit_cast(u32x4, v), srd, voff + off, 0, AUX); }

template <int N> DI void wait_bar() {
 asm volatile("s_waitcnt vmcnt(%0) lgkmcnt(0)\n\ts_barrier" :: "n"(N) : "memory"); }

DI float xmax4(float v) {
  auto a = __builtin_amdgcn_permlane32_swap(__float_as_uint(v), __float_as_uint(v), false, false);
  v = fmaxf(__uint_as_float(a[0]), __uint_as_float(a[1]));
  auto b = __builtin_amdgcn_permlane16_swap(__float_as_uint(v), __float_as_uint(v), false, false);
  return fmaxf(__uint_as_float(b[0]), __uint_as_float(b[1]));
}
DI float xsum4(float v) {
  auto a = __builtin_amdgcn_permlane32_swap(__float_as_uint(v), __float_as_uint(v), false, false);
  v = __uint_as_float(a[0]) + __uint_as_float(a[1]);
  auto b = __builtin_amdgcn_permlane16_swap(__float_as_uint(v), __float_as_uint(v), false, false);
  return __uint_as_float(b[0]) + __uint_as_float(b[1]);
}
DI bf16x8 pack_nat(f32x4 x, f32x4 y, float sc) {
  unsigned x0 = cvtpk(x[0] * sc, x[1] * sc), x1 = cvtpk(x[2] * sc, x[3] * sc);
  unsigned y0 = cvtpk(y[0] * sc, y[1] * sc), y1 = cvtpk(y[2] * sc, y[3] * sc);
  auto p0 = __builtin_amdgcn_permlane32_swap(x0, y0, false, false);
  auto p1 = __builtin_amdgcn_permlane32_swap(x1, y1, false, false);
  auto q0 = __builtin_amdgcn_permlane16_swap(p0[0], p0[1], false, false);
  auto q1 = __builtin_amdgcn_permlane16_swap(p1[0], p1[1], false, false);
  u32x4 r = {q0[0], q1[0], q0[1], q1[1]};
  return __builtin_bit_cast(bf16x8, r);
}
template <bool PK = true> DI bf16x8 pack_acc(f32x4 x, f32x4 y, float sc) {
  auto mu = [&](float a) { return PK ? a * sc : mul1(a, sc); };
  u32x4 r = {cvtpk(mu(x[0]), mu(x[1])), cvtpk(mu(x[2]), mu(x[3])), cvtpk(mu(y[0]), mu(y[1])), cvtpk(mu(y[2]), mu(y[3]))};
  return __builtin_bit_cast(bf16x8, r);
}

template <int I> struct IC { static constexpr int v = I; };
template <typename F, int... Is> DI void sfor_(F&& f, std::integer_sequence<int, Is...>) { (f(IC<Is>{}), ...); }
template <int N, typename F> DI void sfor(F&& f) { sfor_(f, std::make_integer_sequence<int, N>{}); }
#define SB() __builtin_amdgcn_sched_barrier(0)
#define OPAQUE(x) asm volatile("" : "+v"(x))
#define NEED(x) asm volatile("" :: "v"(x))
DI f32x4 zero4() { f32x2 z; asm volatile("v_mov_b64 %0, 0" : "=v"(z)); return __builtin_shufflevector(z, z, 0, 1, 0, 1); }
DI unsigned lane_id() { unsigned l; asm volatile("v_mbcnt_lo_u32_b32 %0, -1, 0\n\tv_mbcnt_hi_u32_b32 %0, -1, %0" : "=v"(l)); return l; }

template <unsigned V> DI void set_m0(unsigned base) {
  asm volatile("s_add_u32 m0, %0, %1" :: "s"(base), "n"(V) : "memory", "scc");
}
template <int OFF, bool NT> DI void piece(unsigned voff, rsrc_t srd, unsigned soff) {
  if constexpr (NT) asm volatile("buffer_load_dwordx4 %0, %1, %2 offen offset:%3 nt lds" :: "v"(voff), "s"(srd), "s"(soff), "n"(OFF) : "memory");
  else asm volatile("buffer_load_dwordx4 %0, %1, %2 offen offset:%3 lds" :: "v"(voff), "s"(srd), "s"(soff), "n"(OFF) : "memory");
}
template <bool NT = false> DI void touch(unsigned voff, rsrc_t srd, unsigned soff) {
  if constexpr (NT) asm volatile("s_mov_b32 m0, %0\n\ts_nop 0\n\tbuffer_load_dword %1, %2, %3 offen nt lds" :: "n"(JUNK), "v"(voff), "s"(srd), "s"(soff) : "memory");
  else asm volatile("s_mov_b32 m0, %0\n\ts_nop 0\n\tbuffer_load_dword %1, %2, %3 offen lds" :: "n"(JUNK), "v"(voff), "s"(srd), "s"(soff) : "memory");
}
#define CNT_W 1
#define CNT_X 0
constexpr unsigned WBIAS = 4096, KBIAS = 1024, VBIAS = 3072;
struct QOff { unsigned w[4], x[2]; };
DI void mk_qoff(QOff& q, unsigned lane) {
  const unsigned r8 = (lane >> 2) & 7, c0 = (lane >> 5) * 64, s0 = ((lane & 3) ^ (2 * ((r8 >> 2) & 1))) * 16;
  const unsigned wa = r8 * 8192 + c0 + s0 + WBIAS, wb = r8 * 8192 + c0 + (s0 ^ 32) + WBIAS + 65536;
  q.w[0] = wa; q.w[1] = wa + 128 - 1024; q.w[2] = wb - 2048; q.w[3] = wb + 128 - 3072;
  const unsigned xa = r8 * 1280 + c0 + s0;
  q.x[0] = xa; q.x[1] = xa + 9216;
}
template <int W, unsigned BUF, int I, bool NT = false> DI void w_issue(const QOff& q, unsigned hb, rsrc_t srdW, unsigned sW) {
  if constexpr (I == 0) set_m0<BUF + 4096 * W>(hb); else piece<1024 * (I - 1), NT>(q.w[I - 1], srdW, sW);
}
template <int W, unsigned AREA, int I, bool NT = false> DI void x_issue(const QOff& q, unsigned hf, rsrc_t srdX, unsigned sC) {
  if constexpr (I == 0) set_m0<AREA + 2048 * W>(hf * 8192);
  else if constexpr (I <= 2) { unsigned t; asm volatile("s_add_u32 %0, %1, %2" : "=s"(t) : "s"(sC + 20480 * W), "s"(hf * 81920) : "scc"); piece<1024 * (I - 1), NT>(q.x[I - 1], srdX, t); }
  else if constexpr (I == 3 && W < 2) {
    if (hf == (AREA >= HALF ? 1u : 0u)) { unsigned t = sC + (16 + W) * 10240; asm volatile("s_mov_b32 m0, %1" : "+s"(t) : "n"(xtail(AREA) + 1024 * W) : "memory"); asm volatile("s_nop 0"); piece<0, NT>(q.x[0], srdX, t); }
  }
}

struct K1State {
  unsigned kb, vb0, vb1, kv0, kv1, hb;
  bf16x8 Qf[3][4];
  f32x4 O[3][8];
  f32x4 negm[3];
  f32x2 l[3];
  f32x4 S[2][2][3];
  unsigned P[2][3][4];
  float e[3][4];
  bf16x8 fr[4];
};
template <int W, unsigned SLOT, int I> DI void kv_issue(const K1State& c, rsrc_t srd0, unsigned s0, rsrc_t srd1, unsigned s1) {
  if constexpr (I == 0) set_m0<SLOT + 4096 * W>(c.hb);
  else if constexpr (I <= 2) piece<1024 * (I - 1), true>((I & 1) ? c.kv0 : c.kv1, srd0, s0);
  else if constexpr (I <= 4) piece<1024 * (I - 1), true>((I & 1) ? c.kv0 : c.kv1, srd1, s1);
}
template <int W, unsigned SLOT> DI void kv_issue_all(const K1State& c, rsrc_t srd0, unsigned s0, rsrc_t srd1, unsigned s1) {
  kv_issue<W, SLOT, 0>(c, srd0, s0, srd1, s1); asm volatile("s_nop 0");
  sfor<4>([&](auto i) { kv_issue<W, SLOT, i.v + 1>(c, srd0, s0, srd1, s1); });
}

constexpr int PFX = 6, QBA = 9, QBAR = 27;
template <int W, unsigned BUF> DI bf16x8 w_frag(unsigned wb0, unsigned wb1, int s, int e) {
  const unsigned o = BUF + 8192 * s + 512 * W;
  return cat(lds_tr((e ? wb1 : wb0) + o), lds_tr((e ? wb0 : wb1) + o + 256));
}
struct QAd { unsigned xl, xh, wb0, wb1; };
template <unsigned AREA> DI bf16x8 x_frag(const QAd& a, int j) {
  const unsigned o = (j % 9 == 8 ? xtail(AREA) : AREA + 2048 * (j % 9)) % HALF - 16384 + 512 * (j / 9);
  return lds_b128((AREA >= HALF ? a.xh : a.xl) + o); }
struct QFr { bf16x8 w[2], x[PFX]; };
template <int W, unsigned BUF, unsigned AREA, int I> DI void q_pre(QFr& f, const QAd& a) {
  if constexpr (I == 0) f.w[0] = w_frag<W, BUF>(a.wb0, a.wb1, 0, 0);
  else if constexpr (I == 1) f.x[0] = x_frag<AREA>(a, 0);
  else if constexpr (I == 2) f.w[1] = w_frag<W, BUF>(a.wb0, a.wb1, 0, 1);
  else if constexpr (I < PFX + 2) f.x[I - 2] = x_frag<AREA>(a, I - 2);
}
template <int W, unsigned BUF, unsigned AREA, bool Z, int VM, typename HK, typename PR>
DI void q_stage(const QFr& f, const QAd& a, f32x4 (&acc)[9][2], HK&& hook, PR&& pre, int more = 0) {
  bf16x8 xf[18], w1[2];
  sfor<PFX>([&](auto i) { xf[i.v] = f.x[i.v]; });
  sfor<36>([&](auto mm) {
    constexpr int m = mm.v, j = m >> 1, e = m & 1, s = j / 9, t = j % 9;
    if constexpr ((m & 3) == 0) { NEED(xf[j + 1]); if constexpr (m == 0) NEED(f.w[1]); }
    MFX<Z && s == 0>(acc[t][e], s ? w1[e] : f.w[e], xf[j]);
    if constexpr (e == 0 && j + PFX < 18) xf[j + PFX] = x_frag<AREA>(a, j + PFX);
    if constexpr (m == 1) w1[0] = w_frag<W, BUF>(a.wb0, a.wb1, 1, 0);
    if constexpr (m == 3) w1[1] = w_frag<W, BUF>(a.wb0, a.wb1, 1, 1);
    SB();
    if constexpr (m == QBA) {
      NEED(w1[0]); NEED(w1[1]);
      asm volatile("s_barrier" ::: "memory");
    }
    if constexpr (m == QBAR) { if (more == 4) wait_bar<VM + 4>(); else if (more == 1) wait_bar<VM + 1>(); else wait_bar<VM>(); }
    if constexpr (m >= QBAR) { pre(IC<m - QBAR>{}); SB(); }
    hook(mm);
    SB();
  });
}

struct SmOp { int kind, u, i; };
constexpr int SMN = 8;
constexpr SmOp sm_dec(int NQ, int K) {
  const int TL = 4 + (NQ - 1) * 8, uu = 1 + (K - 4) / 8, r = (K - 4) % 8;
  if (K < 4) return {0, 0, K};
  if (K >= TL) return {((K - TL) & 1) ? 2 : 1, NQ - 1, (K - TL) >> 1};
  if (r < 2) return {0, uu, r};
  if (r >= 6) return {0, uu, r - 4};
  return {(r & 1) ? 2 : 1, uu - 1, (r - 2) >> 1};
}
#define CLDS 14
#define CBAR 14
#define CWT 7
#define CDMA 14
#define CEXP 20
#define CCVT 14
#define CADD 16
constexpr int sm_cost(int NQ, int K) { const int k = sm_dec(NQ, K % (SMN * NQ)).kind; return k == 0 ? CEXP : k == 1 ? CCVT : CADD; }
template <int NQ, int G, int KK> DI void sm_op(K1State& c) {
  constexpr int T = KK / (SMN * NQ);
  constexpr SmOp o = sm_dec(NQ, KK % (SMN * NQ));
  constexpr int u = o.u, i = o.i;
  if constexpr (o.kind == 0) { c.e[u][i] = __builtin_amdgcn_exp2f(c.S[G][T][u][i]); asm volatile("" : "+v"(c.e[u][i])); }
  else if constexpr (o.kind == 1) { c.P[G][u][2 * T + i] = cvtpk(c.e[u][2 * i], c.e[u][2 * i + 1]); asm volatile("" : "+v"(c.P[G][u][2 * T + i])); }
  else { float a = c.l[u][0], b = c.l[u][1]; asm volatile("v_add_f32 %0, %0, %2\n\tv_add_f32 %1, %1, %3" : "+v"(a), "+v"(b) : "v"(c.e[u][2 * i]), "v"(c.e[u][2 * i + 1])); c.l[u] = f32x2{a, b}; }
}
struct Plan { int start[64]; };
constexpr int unit_gaps(int kind, int nq) { return kind == 0 ? 0 : 8 * nq; }
constexpr int gap_cost(int kind, int nq, bool dm, int m) {
  int c = 0;
  const int f = m / nq, qi = m % nq;
  if (qi == 0) { if (kind == 1) c += f < 4 ? 2 * CLDS : CLDS; else if (kind == 2) c += f < 4 ? CLDS : 2 * CLDS; else c += f < 4 ? CLDS : f >= 6 ? 2 * CLDS : 0; }
  if (kind == 3 && ((f == 5 && qi == nq - 1) || (f == 6 && qi == 1))) c += 2 * CLDS;
  if (kind == 3 && f == 5 && qi == nq - 1) c += CBAR;
  if (qi == nq - 1 && (f & 1)) c += CWT;
  if (dm && qi == 1 && !(f & 1)) c += CDMA;
  if (dm && m == 0) c += CWT;
  return c;
}
constexpr Plan make_plan(int ka, int na, bool da, int kb, int nb, bool db, int ns, int g0, int g1) {
  Plan p{};
  const int ga = unit_gaps(ka, na), G = ga + unit_gaps(kb, nb) - 1, n = 2 * SMN * ns;
  int base[64] = {}, tot = 0;
  for (int g = 0; g < G; ++g) { base[g] = g < ga ? gap_cost(ka, na, da, g) : gap_cost(kb, nb, db, g - ga); if (g >= g0) tot += base[g]; }
  for (int k = 0; k < n; ++k) tot += sm_cost(ns, k);
  int k = 0, cum = 0;
  for (int g = 0; g < 64; ++g) {
    p.start[g] = k;
    if (g < g0 || g >= G) continue;
    cum += base[g];
    const int target = tot * (g - g0 + 1) / (G - g0);
    while (k < n && (g == G - 1 || ((k < SMN * ns || g >= g1) && cum + sm_cost(ns, k) / 2 <= target))) { cum += sm_cost(ns, k); ++k; }
  }
  return p;
}
template <int KA, int NA, bool DA, int KB, int NB, bool DB, int NS, int G, int G0, int G1> struct Win {
  static constexpr Plan pl = make_plan(KA, NA, DA, KB, NB, DB, NS, G0, G1);
  static constexpr int GA = unit_gaps(KA, NA);
  template <int M> static DI void gap(K1State& c) { sfor<pl.start[M + 1] - pl.start[M]>([&](auto k) { sm_op<NS, G, pl.start[M] + k.v>(c); }); }
};
DI bf16x8 pvec(const unsigned (&w)[4]) { u32x4 r = {w[0], w[1], w[2], w[3]}; return __builtin_bit_cast(bf16x8, r); }
template <unsigned AD> DI bf16x8 k_frag(const K1State& c, int f) {
  return lds_b128(c.kb + AD + 8192 * (f >> 2) + 512 * (f & 3)); }
template <unsigned AD> DI bf16x8 v_frag(const K1State& c, int dt) {
  const unsigned o = AD + 512 * (dt >> 1);
  const unsigned vb = (dt & 1) ? c.vb1 : c.vb0;
  return cat(lds_tr(vb + o), lds_tr(vb + o + 8192));
}

template <unsigned SK_, int J, int NQ, typename WIN, int M0, int BAR, int F0, typename NX, typename HK> DI void qk_unit(K1State& c, NX&& nx, HK&& hk) {
  bf16x8 kf[8];
  sfor<4>([&](auto i) { kf[F0 + i.v] = c.fr[i.v]; });
  sfor<8 - F0>([&](auto ff) {
    constexpr int f = ff.v + F0, t = f >> 2, s = f & 3;
    if constexpr ((f & 1) == 0) NEED(kf[f + 1]);
    sfor<NQ>([&](auto q) {
      constexpr int qi = q.v;
      if constexpr (s == 0) MF0(c.S[J][t][qi], kf[f], c.Qf[qi][s], c.negm[qi]); else MF(c.S[J][t][qi], kf[f], c.Qf[qi][s]);
      if constexpr (qi == 0) { if constexpr (f < 4) kf[f + 4] = k_frag<SK_>(c, f + 4); else if constexpr (BAR < 0) c.fr[f - 4] = nx(IC<f - 4>{}); }
      if constexpr (BAR >= 0) {
        if constexpr (f == 5 && qi == NQ - 1) { SB(); wait_bar<BAR>(); c.fr[0] = nx(IC<0>{}); }
        if constexpr (f == 6 && qi < 2) c.fr[1 + qi] = nx(IC<1 + qi>{});
        if constexpr (f == 7 && qi == 0) c.fr[3] = nx(IC<3>{});
      }
      SB();
      if constexpr (qi == 1) { hk(IC<f>{}); SB(); }
      WIN::template gap<M0 + f * NQ + qi>(c);
      SB();
    });
  });
}
template <unsigned SV_, int J, int NQ, typename WIN, int M0, int Z, typename NX, typename HK> DI void pv_unit(K1State& c, NX&& nx, HK&& hk) {
  bf16x8 vf[8], Pb[3];
  sfor<4>([&](auto i) { vf[i.v] = c.fr[i.v]; });
  sfor<NQ>([&](auto q) { Pb[q.v] = pvec(c.P[J][q.v]); });
  sfor<8>([&](auto dd) {
    constexpr int d = dd.v;
    if constexpr ((d & 1) == 0) NEED(vf[d + 1]);
    sfor<NQ>([&](auto q) {
      constexpr int qi = q.v;
      MFX<(Z >> qi) & 1>(c.O[qi][d], vf[d], Pb[qi]);
      if constexpr (qi == 0) { if constexpr (d < 4) vf[d + 4] = v_frag<SV_>(c, d + 4); else c.fr[d - 4] = nx(IC<d - 4>{}); }
      SB();
      if constexpr (qi == 0 && d == 0) { hk(IC<-1>{}); SB(); }
      if constexpr (qi == 1) { hk(dd); SB(); }
      WIN::template gap<M0 + d * NQ + qi>(c);
      SB();
    });
  });
}
template <unsigned SV_, int J, int NQ, typename HK> DI void pv_last(K1State& c, HK&& hk) {
  bf16x8 vf[8], Pb[3];
  sfor<4>([&](auto i) { vf[i.v] = c.fr[i.v]; vf[i.v + 4] = v_frag<SV_>(c, i.v + 4); });
  sfor<NQ>([&](auto q) { Pb[q.v] = pvec(c.P[J][q.v]); });
  SB();
  sfor<8 * NQ>([&](auto mm) {
    constexpr int m = mm.v, d = m & 7, qi = NQ == 3 ? (m / 8 + 2) % 3 : m / 8;
    if constexpr (m < 8 && (d & 1) == 0) NEED(vf[d + 1]);
    MF(c.O[qi][d], vf[d], Pb[qi]);
    SB();
    hk(mm);
    SB();
  });
}
struct NoWin { template <int M> static DI void gap(K1State&) {} };

constexpr unsigned slot_of(int a) { return ((a + 1) & 3) == 0 ? SL1 : ((a + 1) & 3) == 1 ? SL0 : ((a + 1) & 3) == 2 ? SL2 : SL3; }
template <int W, int A, int BAR, typename H0> DI void hstep(K1State& c, H0&& h0) {
  constexpr unsigned SC = slot_of(A), SN = slot_of(A + 1);
  constexpr int N0 = W == (A & 3) ? 3 : 2, N1 = W == ((A + 1) & 3) ? 3 : 2, N2 = W == ((A + 2) & 3) ? 3 : 2, J = A & 1;
  using WN = Win<1, N0, true, 3, N2, false, N1, 1 - J, 0, 5>;
  pv_unit<SC + 2048, J, N0, WN, 0, 0>(c, [&](auto i) { return k_frag<SC>(c, i.v); }, h0);
  qk_unit<SC, J, N2, WN, WN::GA, BAR, 0>(c, [&](auto i) { return v_frag<SN + 2048>(c, i.v); }, [&](auto ff) { h0(IC<ff.v + 8>{}); });
}

struct Ptrs { const char* x[2]; const char* k[2]; const char* v[2]; const char* wq[2]; const char* wo[2]; char* o; char* out; };

constexpr unsigned exch(int t, int s) { const int e = t < 8 ? 3 * t + (s < (t >> 1) ? s : s - 1) : 24 + s; return SL3 + 1024 * e; }

#define T2NT 0
template <int W> DI void k1_wave(const Ptrs& p, const unsigned hf) {
  K1State c;
  const unsigned bid = blockIdx.x, xcd = bid & 7, loc = bid >> 3;
  const unsigned br = xcd >> 2, h0 = ((xcd >> 1) & 1) * 16 + (loc & 3) * 2 + hf, b = (xcd & 1) * 8 + (loc >> 2);
  const char* const pwq = br ? p.wq[1] : p.wq[0];
  const char* const px = br ? p.x[1] : p.x[0];
  const char* const pk = (br ? p.k[1] : p.k[0]) + ((size_t)b * NH + h0) * (SK * DH * 2);
  const char* const pv = (br ? p.v[1] : p.v[0]) + ((size_t)b * NH + h0) * (SK * DH * 2);
  const rsrc_t srdW = mkrsrc(pwq - WBIAS, DM * HD * 2 + WBIAS), srdX = mkrsrc(px + (size_t)b * SQ * DM * 2, SQ * DM * 2);
  constexpr unsigned WOFF = 16 * W * 8192;
  c.hb = hf * HALF;
  const unsigned wi = loc * 8 + hf * 4 + W;
#define GO(a) ((unsigned)(a) * 8192 + 2048 * W)
#define NOP0() asm volatile("s_nop 0")
  {
    const unsigned lane = lane_id();
    QOff q; mk_qoff(q, lane);
    sfor<5>([&](auto i) { w_issue<W, SL0, i.v, CNT_W>(q, c.hb, srdW, h0 * 256 + WOFF); if constexpr (i.v == 0) NOP0(); });
    sfor<4>([&](auto i) { x_issue<W, XA0, i.v, CNT_X>(q, hf, srdX, 0); if constexpr (i.v == 0) NOP0(); });
    sfor<5>([&](auto i) { w_issue<W, SL2, i.v, CNT_W>(q, c.hb, srdW, h0 * 256 + WOFF + 64 * 8192); if constexpr (i.v == 0) NOP0(); });
    sfor<4>([&](auto i) { x_issue<W, XA1, i.v, CNT_X>(q, hf, srdX, 128); if constexpr (i.v == 0) NOP0(); });
    const bool ww = wi < 128;
    const unsigned xi = wi - 128;
    {
      const unsigned tv = ww ? (lane >> 4) * 8192 + (lane & 15) * 128 : lane * 1280, ts = ww ? (wi + 32) * 32768 + (h0 & 16) * 256 : (xi % 18) * 81920 + (xi / 18 + 2) * 128;
      const rsrc_t tr = mkrsrc(ww ? pwq : px + (size_t)(xcd & 1) * 8 * SQ * DM * 2, !WARM ? 0 : ww ? DM * HD * 2 : 8 * SQ * DM * 2);
      if (CNT_W == CNT_X) touch<CNT_W>(tv, tr, ts); else if (ww) touch<CNT_W>(tv, tr, ts); else touch<CNT_X>(tv, tr, ts);
    }
    touch<CNT_X>(lane * 1280, mkrsrc(px + (size_t)(xcd & 1) * 8 * SQ * DM * 2, WARM && wi < 16 ? 8 * SQ * DM * 2 : 0), (wi + 2) * 81920 + 9 * 128);
  }
  bf16x8 ob[9];
  rsrc_t srdP = mkrsrc(p.o, 0);
#define OB_UNDEF() asm volatile("" : "=v"(ob[0]), "=v"(ob[1]), "=v"(ob[2]), "=v"(ob[3]), "=v"(ob[4]), "=v"(ob[5]), "=v"(ob[6]), "=v"(ob[7]), "=v"(ob[8]))
#define OB_STORE(I, WA) OB_STORE_(OST1_AUX, I, WA)
#define OB_STORE_(AUX, I, WA) ost<AUX>(srdP, WA, (I) < 8 ? 8192 * W + 1024 * (I) : 32768 + 1024 * W, ob[I])
  OB_UNDEF();
  if (hf) __builtin_amdgcn_s_setprio(1);
#define TILE(a) srdK, GO((a) + 2), srdV, GO(a)
#define QP(BUF, AREA, F) [&](auto i) { q_pre<W, BUF, AREA, i.v>(F, a); }
#define QH(HD, AH, SH, BW, SW, AT, ST_) [&](auto mm) { constexpr int m = mm.v; \
        if constexpr (m == 5 && HD) x_issue<W, AH, 3>(q, hf, srdX, SH); \
        if constexpr (m == QBA) w_issue<W, BW, 0>(q, c.hb, srdW, SW); \
        if constexpr (m > QBA && m <= QBA + 14 && (m - QBA) % 4 == 2) w_issue<W, BW, 1 + (m - QBA) / 4>(q, c.hb, srdW, SW); \
        if constexpr (m == QBAR) x_issue<W, AT, 0>(q, hf, srdX, ST_); \
        if constexpr (m == QBAR + 3) x_issue<W, AT, 1>(q, hf, srdX, ST_); \
        if constexpr (m == 35) x_issue<W, AT, 2>(q, hf, srdX, ST_); }
#pragma unroll 1
  for (int it = 0; it < 2; ++it) {
    const unsigned h = h0 + 8 * it;
    const unsigned sW0 = h * 256 + WOFF;
    const rsrc_t srdK = mkrsrc(pk + (size_t)it * 8 * (SK * DH * 2) - KBIAS, SK * DH * 2 + KBIAS), srdV = mkrsrc(pv + (size_t)it * 8 * (SK * DH * 2) - VBIAS, SK * DH * 2 + VBIAS);
    {
      const unsigned lane = lane_id();
      const unsigned rr = lane & 15, g = lane >> 4, qq = (lane & 15) >> 2, pp = lane & 3, r8 = (lane >> 2) & 7;
      c.kb = 4096 * (rr >> 3) + 64 * (rr & 7) + 16 * (g ^ (2 * ((rr >> 2) & 1))) + c.hb;
      c.vb0 = 4096 * (g >> 1) + 64 * (4 * (g & 1) + qq) + 16 * ((pp >> 1) ^ (2 * (g & 1))) + 8 * (pp & 1) + c.hb;
      c.vb1 = c.vb0 ^ 32;
      c.kv1 = r8 * 256 + (lane >> 5) * 64 + (((lane & 3) ^ (2 * ((r8 >> 2) & 1))) * 16) + 128;
      c.kv0 = c.kv1 + (KBIAS - 128);
      OPAQUE(c.kb); OPAQUE(c.vb0); OPAQUE(c.vb1); OPAQUE(c.kv0); OPAQUE(c.kv1);
      QAd a;
      a.xl = 1024 * (rr >> 3) + 64 * (rr & 7) + 16 * (g ^ (2 * ((rr >> 2) & 1))) + 16384;
      a.xh = a.xl + HALF;
      a.wb0 = 2048 * g + 64 * qq + 16 * ((pp >> 1) ^ (2 * (g & 1))) + 8 * (pp & 1) + c.hb;
      a.wb1 = a.wb0 ^ 32;
      QOff q; mk_qoff(q, lane);
      unsigned wa = 16 * lane + 8192 * W;
      OPAQUE(a.xl); OPAQUE(a.xh); OPAQUE(a.wb0); OPAQUE(a.wb1); OPAQUE(wa);
      OPAQUE(q.w[0]); OPAQUE(q.w[1]); OPAQUE(q.w[2]); OPAQUE(q.w[3]); OPAQUE(q.x[0]); OPAQUE(q.x[1]);
      f32x4 acc[9][2];
      QFr fa, fb;
      if (it == 0) {
        wait_bar<6 + NT0>();
        sfor<PFX + 2>([&](auto i) { q_pre<W, SL0, XA0, i.v>(fa, a); SB(); });
        q_stage<W, SL0, XA0, true, 4 + NT0>(fa, a, acc, QH(false, XA1, 0, SL0, sW0 + 2 * 64 * 8192, XB0, 256), QP(SL2, XA1, fb));
      } else {
        wait_bar<1>();
        sfor<PFX + 2>([&](auto i) { q_pre<W, SL0, XA0, i.v>(fa, a); SB(); });
        sfor<5>([&](auto i) { w_issue<W, SL2, i.v>(q, c.hb, srdW, sW0 + 64 * 8192); if constexpr (i.v == 0) NOP0(); });
        SB();
        auto qh0 = QH(false, XA1, 128, SL0, sW0 + 2 * 64 * 8192, XB0, 256);
        q_stage<W, SL0, XA0, true, 5>(fa, a, acc, [&](auto mm) { qh0(mm); if constexpr (mm.v == 7) ost<OST1_AUX>(srdP, wa, 32768 - 7168 * W, ob[8]); }, QP(SL2, XA1, fb));
      }
      {
        auto qh1 = QH(true, XB0, 256, SL2, sW0 + 3 * 64 * 8192, XB1, 384);
        q_stage<W, SL2, XA1, false, 5>(fb, a, acc, [&](auto mm) { qh1(mm); if constexpr (mm.v == 7) ost<OST1_AUX>(srdP, wa, 7168, ob[7]); }, QP(SL0, XB0, fa));
      }
#pragma unroll 1
      for (int j = 2; j < 8; j += 2) {
        unsigned sw = sW0 + (j + 2) * 64 * 8192, sw2 = sw + 64 * 8192, sx = (j + 1) * 128, sx2 = sx + 128, sx3 = sx + 256;
        asm volatile("" : "+s"(sw), "+s"(sw2), "+s"(sx), "+s"(sx2), "+s"(sx3));
        auto qhe = QH(true, XB1, sx, SL0, sw, XB0, sx2);
        auto qho = QH(true, XB0, sx2, SL2, sw2, XB1, sx3);
        q_stage<W, SL0, XB0, false, 5>(fa, a, acc, [&](auto mm) { qhe(mm); if constexpr (mm.v == 7) ost<OST1_AUX>(srdP, wa, 0, ob[0]); }, QP(SL2, XB1, fb));
        q_stage<W, SL2, XB1, false, 5>(fb, a, acc, [&](auto mm) {
          constexpr int m = mm.v;
          qho(mm);
          if constexpr (m == 7) ost<OST1_AUX>(srdP, wa, 1024, ob[1]);
          if constexpr (m >= 12 && m <= 20 && !(m & 1)) { ob[(m - 12) / 2] = ob[(m - 12) / 2 + 2]; OPAQUE(ob[(m - 12) / 2]); }
          if constexpr (m == 22) { wa += 2048; OPAQUE(wa); }
        }, QP(SL0, XB0, fa));
      }
      q_stage<W, SL0, XB0, false, 9>(fa, a, acc, [&](auto mm) {
        constexpr int m = mm.v;
        if constexpr (m == 1) x_issue<W, XB1, 3>(q, hf, srdX, 9 * 128);
        if constexpr (m == 2) ost<OST1_AUX>(srdP, wa, 0, ob[0]);
        if constexpr (m >= 3 && m <= 11 && (m & 1)) kv_issue<W, SL1, (m - 3) / 2>(c, srdK, GO(0), srdK, GO(1) - 2048);
        if constexpr (m >= 13 && m <= 25 && (m - 13) % 3 == 0) kv_issue<W, SL0, (m - 13) / 3>(c, TILE(0));
      }, QP(SL2, XB1, fb));
      q_stage<W, SL2, XB1, false, 12>(fb, a, acc, [&](auto mm) {
        constexpr int m = mm.v;
      }, [](auto) {});
      MFMA_FENCE();
#pragma unroll
      for (int i = 0; i < 9; ++i) asm volatile("" : "+v"(acc[i][0]), "+v"(acc[i][1]));
      unsigned qa = 16 * lane_id() + 16384 + c.hb; OPAQUE(qa);
      sfor<9>([&](auto tt) {
        constexpr int t = tt.v;
        const bf16x8 f = pack_nat(acc[t][0], acc[t][1], QSCALE);
        if constexpr ((t >> 1) == W) c.Qf[t & 1][W] = f; else *(LDSP bf16x8*)(size_t)(qa + exch(t, W) - 16384) = f;
      });
      kv_issue_all<W, SL2>(c, TILE(1));
      wait_bar<8>();
      sfor<4>([&](auto i) { c.fr[i.v] = k_frag<SL1>(c, i.v); });
      sfor<4>([&](auto s) {
        if constexpr (s.v != W) { c.Qf[0][s.v] = lds_b128(qa + exch(2 * W, s.v) - 16384); c.Qf[1][s.v] = lds_b128(qa + exch(2 * W + 1, s.v) - 16384); }
        c.Qf[2][s.v] = lds_b128(qa + exch(8, s.v) - 16384);
      });
    }
    const rsrc_t srdWn = mkrsrc(pwq - WBIAS, it == 0 ? DM * HD * 2 + WBIAS : 0), srdXn = mkrsrc(px + (size_t)b * SQ * DM * 2, it == 0 ? SQ * DM * 2 : 0);
#pragma unroll
    for (int qi = 0; qi < 3; ++qi) c.negm[qi] = zero4();
    bool redo = false;
#pragma unroll 1
    for (;;) {
#pragma unroll
      for (int qi = 0; qi < 3; ++qi) {
        c.O[qi][0] = zero4();
        c.l[qi] = f32x2{c.O[qi][0][0], c.O[qi][0][1]};
#pragma unroll
        for (int dt = 1; dt < 8; ++dt) c.O[qi][dt] = c.O[qi][0];
        asm volatile("" : "+v"(c.l[qi]), "+v"(c.O[qi][0]), "+v"(c.O[qi][1]), "+v"(c.O[qi][2]), "+v"(c.O[qi][3]), "+v"(c.O[qi][4]), "+v"(c.O[qi][5]), "+v"(c.O[qi][6]), "+v"(c.O[qi][7]));
      }
      asm volatile("s_nop 1" : "+v"(c.negm[0]), "+v"(c.negm[1]), "+v"(c.negm[2]));
      SB();
      {
        constexpr int N0 = W == 0 ? 3 : 2, N1 = W == 1 ? 3 : 2;
        bf16x8 kf[4];
        sfor<4>([&](auto i) { kf[i.v] = c.fr[i.v]; });
        sfor<4>([&](auto s) {
          sfor<3>([&](auto q) {
            if constexpr (s.v == 0) MF0(c.S[0][0][q.v], kf[0], c.Qf[q.v][0], c.negm[q.v]); else MF(c.S[0][0][q.v], kf[s.v], c.Qf[q.v][s.v]);
            if constexpr (q.v == 0) c.fr[s.v] = k_frag<SL1>(c, 4 + s.v);
            SB();
          });
        });
        asm volatile("s_barrier" ::: "memory");
        MFMA_FENCE();
        asm volatile("" : "+v"(c.S[0][0][0]), "+v"(c.S[0][0][1]), "+v"(c.S[0][0][2]));
#pragma unroll
        for (int qi = 0; qi < 3; ++qi) {
          const float mx = xmax4(fmaxf(fmaxf(c.S[0][0][qi][0], c.S[0][0][qi][1]), fmaxf(c.S[0][0][qi][2], c.S[0][0][qi][3])));
          const float dl = redo ? 0.f : mx;
          c.negm[qi] -= dl;
          if (qi < N0) c.S[0][0][qi] -= dl;
        }
        asm volatile("s_nop 1" : "+v"(c.negm[0]), "+v"(c.negm[1]), "+v"(c.negm[2]), "+v"(c.S[0][0][0]), "+v"(c.S[0][0][1]));
        SB();
        using WP = Win<2, N0, false, 3, N1, false, N0, 0, 4 * N0, 8 * N0 + 6>;
        qk_unit<SL1, 0, N0, WP, 0, -1, 4>(c, [&](auto i) { return k_frag<SL1 + 2048>(c, i.v); }, [](auto) {});
        qk_unit<SL1 + 2048, 1, N1, WP, WP::GA, 8, 0>(c, [&](auto i) { return v_frag<SL0 + 2048>(c, i.v); }, [&](auto ff) {
          if constexpr (ff.v < 5) kv_issue<W, SL3, ff.v>(c, TILE(2)); });
      }
#define KVH(SLOT, SRD0, S0, SRD1, S1) [&](auto ff) { constexpr int f = ff.v; \
        if constexpr (f == -1) kv_issue<W, SLOT, 0>(c, SRD0, S0, SRD1, S1); \
        if constexpr (f >= 0 && f < 8 && !(f & 1)) kv_issue<W, SLOT, 1 + f / 2>(c, SRD0, S0, SRD1, S1); }
#pragma unroll 1
      for (int u = 0; u < 3; ++u) {
        unsigned t3 = GO(4 * u + 3), t4 = GO(4 * u + 4), t5 = GO(4 * u + 5), t6 = GO(4 * u + 6), t7 = GO(4 * u + 7);
        unsigned t8 = u == 2 ? GO(15) + 2048 : GO(4 * u + 8);
        const rsrc_t srdL = u == 2 ? srdV : srdK;
        asm volatile("" : "+s"(t3), "+s"(t4), "+s"(t5), "+s"(t6), "+s"(t7), "+s"(t8));
        hstep<W, 0, 8>(c, KVH(SL1, srdK, t5, srdV, t3));
        hstep<W, 1, 8>(c, KVH(SL0, srdK, t6, srdV, t4));
        hstep<W, 2, 8>(c, KVH(SL2, srdK, t7, srdV, t5));
        hstep<W, 3, 8>(c, KVH(SL3, srdL, t8, srdV, t6));
      }
      {
        hstep<W, 0, 8>(c, [&](auto ff) {
          constexpr int f = ff.v;
          if constexpr (f >= 0 && f < 8) {
            { QOff q; mk_qoff(q, lane_id()); if constexpr (f < 4) x_issue<W, XA0, f>(q, hf, srdXn, 0); else x_issue<W, XA1, f - 4>(q, hf, srdXn, 128); }
          }
        });
        hstep<W, 1, 8>(c, [&](auto ff) {
          constexpr int f = ff.v;
          if constexpr (f == -1) set_m0<SL0 + 4096 * W>(c.hb);
          if constexpr (f >= 0 && f < 8 && !(f & 1)) {
            { QOff q; mk_qoff(q, lane_id()); piece<1024 * (f / 2), false>(q.w[f / 2], srdWn, sW0 + 2048); }
          }
        });
        constexpr int N0 = W == 2 ? 3 : 2, N2 = W == 3 ? 3 : 2;
        using WL = Win<1, N0, false, 0, 2, false, N2, 1, 0, 5>;
        pv_unit<SL3 + 2048, 0, N0, WL, 0, 0>(c, [&](auto i) { return v_frag<SL3>(c, i.v); }, [&](auto ff) {
          if constexpr (ff.v == -1) {
            const unsigned ln = lane_id();
            touch<T2NT>((ln >> 4) * 8192 + (ln & 15) * 128, mkrsrc(pwq + wi * 32768 + ((h0 & 16) + 8) * 256, WARM && it == 0 && wi < 160 ? 3 * 8192 + 2048 : 0), 0);
          }
        });
      }
      const unsigned lane = lane_id();
      unsigned wl = 16 * lane + c.hb + 16384, wm = 4 * (lane & 15) + c.hb + 16384; OPAQUE(wl); OPAQUE(wm);
      float ls[3], inv0, inv1;
      {
        constexpr int N2 = W == 3 ? 3 : 2, P0 = W == 3 ? 16 : 8, I0 = W == 3 ? 10 : 2;
        asm volatile("s_nop 1");
        pv_last<SL3, 1, N2>(c, [&](auto mm) {
          constexpr int m = mm.v;
          constexpr int LO = 0;
          if constexpr (m < 3) { ls[m] = xsum4(c.l[m][0] + c.l[m][1]); OPAQUE(ls[m]); }
          if constexpr (m == LO + 3) { inv0 = __builtin_amdgcn_rcpf(ls[0]); OPAQUE(inv0); *(LDSP float*)(size_t)(wm + MRG_ML - 16384 + 64 * W) = ls[2]; }
          if constexpr (m == LO + 4) { inv1 = __builtin_amdgcn_rcpf(ls[1]); OPAQUE(inv1); }
          if constexpr (m == LO + 5) { const bool bad = __any(!(ls[0] <= LTHR) || !(ls[1] <= LTHR) || !(ls[2] <= LTHR)); *(LDSP unsigned*)(size_t)(MRG_FL + 16 * hf + 4 * W) = bad ? 1u : 0u; }
          if constexpr (m >= I0 && m < I0 + 6) {
            constexpr int k = m - I0, o = (k >> 1) < W ? (k >> 1) : (k >> 1) + 1, src = W < o ? W : W - 1;
            asm volatile("" : "+v"(c.O[2][2 * o + (k & 1)]));
            *(LDSP f32x4*)(size_t)(wl + mrg_in(o, src) - 16384 + 1024 * (k & 1)) = c.O[2][2 * o + (k & 1)];
          }
          if constexpr (m >= P0 && !(m & 1)) {
            constexpr int sx = (m - P0) / 2;
            asm volatile("" : "+v"(c.O[0][2 * sx]), "+v"(c.O[0][2 * sx + 1]));
            ob[sx] = pack_acc<false>(c.O[0][2 * sx], c.O[0][2 * sx + 1], inv0); OPAQUE(ob[sx]);
          }
        });
      }
      MFMA_FENCE();
      asm volatile("" : "+v"(c.O[1][0]), "+v"(c.O[1][1]), "+v"(c.O[1][2]), "+v"(c.O[1][3]), "+v"(c.O[1][4]), "+v"(c.O[1][5]), "+v"(c.O[1][6]), "+v"(c.O[1][7]), "+v"(c.O[2][2 * W]), "+v"(c.O[2][2 * W + 1]));
#pragma unroll
      for (int sx = 0; sx < 4; ++sx) { ob[4 + sx] = pack_acc(c.O[1][2 * sx], c.O[1][2 * sx + 1], inv1); OPAQUE(ob[4 + sx]); }
      asm volatile("s_waitcnt lgkmcnt(0)\n\ts_barrier" ::: "memory");
      float ml[4];
#pragma unroll
      for (int o = 0; o < 4; ++o) ml[o] = *(LDSP const float*)(size_t)(wm + MRG_ML - 16384 + 64 * o);
      const u32x4 f0 = *(LDSP const u32x4*)(size_t)MRG_FL, f1 = *(LDSP const u32x4*)(size_t)(MRG_FL + 16);
      f32x4 a0 = c.O[2][2 * W], a1 = c.O[2][2 * W + 1];
#pragma unroll
      for (int o = 0; o < 3; ++o) {
        const unsigned ad = mrg_in(W, o) - 16384;
        a0 += *(LDSP const f32x4*)(size_t)(wl + ad);
        a1 += *(LDSP const f32x4*)(size_t)(wl + ad + 1024);
      }
      const float L = (ml[0] + ml[1]) + (ml[2] + ml[3]);
      ob[8] = pack_acc(a0, a1, __builtin_amdgcn_rcpf(L));
      const bool anybad = __builtin_amdgcn_readfirstlane(f0[0] | f0[1] | f0[2] | f0[3] | f1[0] | f1[1] | f1[2] | f1[3]) != 0u;
      if (__builtin_expect(!anybad || redo, 1)) break;
      redo = true;
      float mx[3] = {-INFINITY, -INFINITY, -INFINITY};
#pragma unroll 1
      for (int t = 0; t < 8; ++t) {
        wait_bar<0>();
        kv_issue_all<W, SL0>(c, srdK, t * 16384 + 2048 * W, srdK, t * 16384 + 6144 + 2048 * W);
        wait_bar<0>();
#pragma unroll 1
        for (int a = 0; a < 4; ++a) {
          f32x4 s[3] = {zero4(), zero4(), zero4()};
#pragma unroll
          for (int ks = 0; ks < 4; ++ks) {
            const bf16x8 kf = lds_b128(c.kb + SL0 + 8192 * (a >> 1) + 2048 * (a & 1) + 512 * ks);
#pragma unroll
            for (int qi = 0; qi < 3; ++qi) s[qi] = mfma16(kf, c.Qf[qi][ks], s[qi]);
          }
#pragma unroll
          for (int qi = 0; qi < 3; ++qi) mx[qi] = fmaxf(fmaxf(mx[qi], fmaxf(s[qi][0], s[qi][1])), fmaxf(s[qi][2], s[qi][3]));
        }
      }
#pragma unroll
      for (int qi = 0; qi < 3; ++qi) { const float m = -xmax4(mx[qi]); c.negm[qi] = f32x4{m, m, m, m}; }
      wait_bar<0>();
      kv_issue_all<W, SL1>(c, srdK, GO(0), srdK, GO(1) - 2048); kv_issue_all<W, SL0>(c, TILE(0)); kv_issue_all<W, SL2>(c, TILE(1));
      wait_bar<0>();
      sfor<4>([&](auto i) { c.fr[i.v] = k_frag<SL1>(c, i.v); });
    }
    srdP = __builtin_amdgcn_make_buffer_rsrc(p.o + ((size_t)(br * NH + h) * 144 + b * 9) * 4096, (short)0, 9 * 4096, 0x00020000);
  }
  { const unsigned wa = 16 * lane_id(); sfor<9>([&](auto i) { OB_STORE_(OST_AUX, i.v, wa); }); }
  if constexpr (WOEND == 1) {
    const unsigned ln = lane_id();
    const rsrc_t srdT = mkrsrc((bid >> 7 ? p.wo[1] : p.wo[0]) + (((bid & 127) >> 1) * 8 + hf * 4 + W) * 10240, WARM && !(bid & 1) ? 10240 : 0);
    touch(ln * 128, srdT, 0); touch(ln * 128, srdT, 8192);
  }
  asm volatile("s_waitcnt vmcnt(0)" ::: "memory");
}

__global__ __launch_bounds__(512, 2) void k1_attn(Ptrs p) {
#define WROT 1
  const unsigned w8 = __builtin_amdgcn_readfirstlane(threadIdx.x >> 6), hf = w8 >> 2, w = (w8 + WROT * hf) & 3;
  if (w == 0) k1_wave<0>(p, hf);
  else if (w == 1) k1_wave<1>(p, hf);
  else if (w == 2) k1_wave<2>(p, hf);
  else k1_wave<3>(p, hf);
}

#define GRID 512
constexpr unsigned K2_LDS = 147456;
#define K2NOC 0

template <int OFF> DI void k2_wpiece(unsigned voff, rsrc_t srd, unsigned soff) {
  asm volatile("buffer_load_dwordx4 %0, %1, %2 offen offset:%3 lds" :: "v"(voff), "s"(srd), "s"(soff), "n"(OFF) : "memory");
}
DI void k2_oload(bf16x8& d, unsigned voff, rsrc_t srd, unsigned soff) {
  asm volatile("buffer_load_dwordx4 %0, %1, %2, %3 offen" : "=v"(d) : "v"(voff), "s"(srd), "s"(soff) : "memory");
}
__global__ __launch_bounds__(512, 2) void k2_oproj(Ptrs p) {
  const unsigned lane = threadIdx.x & 63, w = __builtin_amdgcn_readfirstlane(threadIdx.x >> 6);
  const unsigned kg = w & 3, qh = w >> 2;
  const unsigned g = lane >> 4, qq = (lane & 15) >> 2, pp = lane & 3;
  const unsigned bid = blockIdx.x, xcd = bid & 7, loc = bid >> 3;
  const unsigned rbg = xcd * 6 + loc / 5, cb = loc % 5;
  const unsigned br = rbg / 24, rb = rbg % 24;
  const rsrc_t srdW = mkrsrc((br ? p.wo[1] : p.wo[0]) - 1024, HD * DM * 2 + 1024);
  const rsrc_t srdO = mkrsrc(p.o + (size_t)br * NBATCH * 9 * 128 * 1024, NBATCH * 9 * 128 * 1024);
  const unsigned r8 = (lane >> 2) & 7, sub = lane >> 5, sl = lane & 3;
  unsigned vw[4], vo[3];
  { const unsigned vr = r8 * 1280 + ((sl ^ (2 * ((r8 >> 2) & 1))) * 16) + 1024, c0 = (sub ^ (w >> 1)) * 64, c1 = c0 ^ 128;
    vw[0] = vr + c0; vw[1] = vr + c1 - 1024; vw[2] = vr + c0 + 10240 - 2048; vw[3] = vr + c1 + 10240 - 3072; }
  vo[0] = lane * 16; vo[1] = vo[0] + 4096; vo[2] = vo[0] + 8192;
  const unsigned m0b = w * 4096;
  unsigned sw = 16 * w * 1280 + cb * 256, so = (rb * 6 + qh * 3) * 4096 + kg * 1024;
  bf16x8 ob[4][3];
  auto wstep = [&](auto i, unsigned slotoff) {
    if constexpr (i.v == 0) asm volatile("s_mov_b32 m0, %0" :: "s"(m0b + slotoff) : "memory");
    else { k2_wpiece<1024 * (i.v - 1)>(vw[i.v - 1], srdW, sw); if constexpr (i.v == 4) { sw += 128 * 1280; asm volatile("" : "+s"(sw)); } }
  };
  auto ostep = [&](auto i, bf16x8 (&d)[3]) { k2_oload(d[i.v], vo[i.v], srdO, so); if constexpr (i.v == 2) { so += 144 * 4096; asm volatile("" : "+s"(so)); } };
  auto wall = [&](unsigned slotoff) { wstep(IC<0>{}, slotoff); asm volatile("s_nop 0"); sfor<4>([&](auto i) { wstep(IC<i.v + 1>{}, slotoff); }); };
  auto oall = [&](bf16x8 (&d)[3]) { sfor<3>([&](auto i) { ostep(i, d); }); };
  wall(0); wall(32768); oall(ob[0]); wall(65536); oall(ob[1]); wall(98304); oall(ob[2]);
  unsigned vb0 = 2048 * (g >> 1) + 64 * (4 * (g & 1) + qq) + 16 * ((pp >> 1) ^ (2 * (g & 1))) + 8 * (pp & 1) + kg * 8192;
  unsigned vb1 = vb0 ^ 32, vb2 = vb0 + 65536, vb3 = vb1 + 65536;
  OPAQUE(vb0); OPAQUE(vb1); OPAQUE(vb2); OPAQUE(vb3);
  f32x4 acc[8][3];
#pragma unroll
  for (int i = 0; i < 8; ++i) { acc[i][0] = zero4(); acc[i][1] = acc[i][0]; acc[i][2] = acc[i][0]; asm volatile("" : "+v"(acc[i][0]), "+v"(acc[i][1]), "+v"(acc[i][2])); }
  bf16x8 F[2][8];
  auto fread = [&](int slot, int nt) {
    const unsigned o = (slot & 1) * 32768 + 512 * (nt >> 1);
    const unsigned vb = (slot & 2) ? ((nt & 1) ? vb3 : vb2) : ((nt & 1) ? vb1 : vb0);
    return cat(lds_tr(vb + o), lds_tr(vb + o + 4096));
  };
  asm volatile("s_waitcnt vmcnt(21)\n\ts_barrier" ::: "memory");
#pragma unroll
  for (int nt = 0; nt < 8; ++nt) F[0][nt] = fread(0, nt);
  auto period = [&](auto uu, auto nw, auto dow, auto doo, auto dor) {
    constexpr int U = uu.v;
    asm volatile("s_waitcnt vmcnt(%3) lgkmcnt(0)\n\ts_barrier" : "+v"(ob[U][0]), "+v"(ob[U][1]), "+v"(ob[U][2]) : "n"(nw.v) : "memory");
    SB();
    sfor<24>([&](auto mm) {
      constexpr int m = mm.v, nt = m / 3, qi = m % 3;
      if (!K2NOC) MF(acc[nt][qi], F[U & 1][nt], ob[U][qi]);
      if constexpr (dor.v && !K2NOC && m < 16 && (m & 1) == 0) F[(U + 1) & 1][m >> 1] = fread((U + 1) & 3, m >> 1);
      SB();
      if constexpr (dow.v && m == 0) wstep(IC<0>{}, U * 32768);
      if constexpr (dow.v && m >= 1 && m <= 19 && m % 6 == 1) wstep(IC<1 + m / 6>{}, U * 32768);
      if constexpr (doo.v && m >= 4 && m <= 16 && m % 6 == 4) ostep(IC<m / 6>{}, ob[(U + 3) & 3]);
      SB();
    });
  };
#pragma unroll 1
  for (int st = 0; st < 28; st += 4) {
    period(IC<0>{}, IC<14>{}, IC<1>{}, IC<1>{}, IC<1>{});
    period(IC<1>{}, IC<14>{}, IC<1>{}, IC<1>{}, IC<1>{});
    period(IC<2>{}, IC<14>{}, IC<1>{}, IC<1>{}, IC<1>{});
    period(IC<3>{}, IC<14>{}, IC<1>{}, IC<1>{}, IC<1>{});
  }
  period(IC<0>{}, IC<14>{}, IC<0>{}, IC<1>{}, IC<1>{});
  period(IC<1>{}, IC<10>{}, IC<0>{}, IC<0>{}, IC<1>{});
  period(IC<2>{}, IC<3>{}, IC<0>{}, IC<0>{}, IC<1>{});
  const unsigned ln = lane_id();
  unsigned wd[3], rd = (w * 3) * 6144 + ln * 16;
#pragma unroll
  for (int j = 1; j < 4; ++j) { wd[j - 1] = ((w ^ j) * 3 + j - 1) * 6144 + ln * 16; OPAQUE(wd[j - 1]); }
  OPAQUE(rd);
  asm volatile("s_waitcnt vmcnt(0) lgkmcnt(0)\n\ts_barrier" : "+v"(ob[3][0]), "+v"(ob[3][1]), "+v"(ob[3][2]) :: "memory");
  SB();
  sfor<24>([&](auto mm) {
    constexpr int m = mm.v, nt = (m / 3 + 2) & 7, qi = m % 3, pm = m - 5, pn = (pm / 3 + 2) & 7, pq = pm % 3;
    MF(acc[nt][qi], F[1][nt], ob[3][qi]);
    SB();
    if constexpr (pm == 0) asm volatile("s_nop 1");
    if constexpr (pm >= 0 && pm < 18) { asm volatile("" : "+v"(acc[pn][pq])); *(LDSP f32x4*)(size_t)(wd[(pn >> 1) - 1] + ((pn & 1) * 3 + pq) * 1024) = acc[pn][pq]; }
    SB();
  });
  asm volatile("s_waitcnt lgkmcnt(0)\n\ts_barrier\n\ts_nop 7" : "+v"(acc[0][0]), "+v"(acc[0][1]), "+v"(acc[0][2]), "+v"(acc[1][0]), "+v"(acc[1][1]), "+v"(acc[1][2]) :: "memory");
  f32x4 a[2][3];
#pragma unroll
  for (int qi = 0; qi < 3; ++qi)
#pragma unroll
    for (int e = 0; e < 2; ++e) {
      const f32x4 p0 = *(LDSP const f32x4*)(size_t)(rd + (e * 3 + qi) * 1024), p1 = *(LDSP const f32x4*)(size_t)(rd + 6144 + (e * 3 + qi) * 1024), p2 = *(LDSP const f32x4*)(size_t)(rd + 12288 + (e * 3 + qi) * 1024);
      a[e][qi] = (acc[e][qi] + p0) + (p1 + p2);
    }
#pragma unroll
  for (int qi = 0; qi < 3; ++qi) {
    const unsigned row = (rb * 6 + qh * 3 + qi) * 16 + (ln & 15);
    *(bf16x8*)(p.out + (size_t)row * (2 * DM * 2) + (br * DM + cb * 128 + kg * 32 + (ln >> 4) * 8) * 2) = pack_nat(a[0][qi], a[1][qi], 1.f);
  }
}

extern "C" void kernel_launch(void* const* d_in, const int* in_sizes, int n_in, void* d_out, int out_size, void* d_ws, size_t ws_size, hipStream_t stream) {
  Ptrs p;
  for (int i = 0; i < 2; ++i) {
    p.x[i] = (const char*)d_in[5 * i + 0]; p.k[i] = (const char*)d_in[5 * i + 1]; p.v[i] = (const char*)d_in[5 * i + 2];
    p.wq[i] = (const char*)d_in[5 * i + 3]; p.wo[i] = (const char*)d_in[5 * i + 4];
  }
  p.o = (char*)d_ws; p.out = (char*)d_out;
  (void)hipFuncSetAttribute((const void*)k1_attn, hipFuncAttributeMaxDynamicSharedMemorySize, K1_LDS);
  (void)hipFuncSetAttribute((const void*)k2_oproj, hipFuncAttributeMaxDynamicSharedMemorySize, K2_LDS);
  hipLaunchKernelGGL(k1_attn, dim3(256), dim3(512), K1_LDS, stream, p);
  hipLaunchKernelGGL(k2_oproj, dim3(240), dim3(512), K2_LDS, stream, p);
}
```
